```python
import torch

def _softmax(x, axis=-1):
    return torch.softmax(x, dim=axis)
def _gelu(x):
    return torch.nn.functional.gelu(x, approximate="tanh")
def _silu(x):
    return torch.nn.functional.silu(x)
def _sigmoid(x):
    return torch.sigmoid(x)
def _layernorm(x, axis=-1, eps=1e-5):
    m = x.mean(dim=axis, keepdim=True)
    v = x.var(dim=axis, keepdim=True, unbiased=False)
    return (x - m) / torch.sqrt(v + eps)
def _scatter_add(tgt, idx, src, axis=0):
    return tgt.clone().index_add_(axis, idx, src)
def _segment_sum(src, seg, n):
    out = torch.zeros((n,) + src.shape[1:], dtype=src.dtype)
    return out.index_add_(0, seg, src)
def _pad(x, pw):
    flat = tuple(v for pair in reversed(pw) for v in pair)
    return torch.nn.functional.pad(x, flat)

kernel_name = 'concat_heads__linear_attn_block__ssm_conv_h3__prompt'

def setup_inputs():

    _w = (lambda a: (lambda t: t.to(torch.bfloat16) if t.is_floating_point() else t)(torch.as_tensor(a)))
    def _A_setup():
        B, S, D = 1, 2048, 1024
        return {
            "x": _w((lambda *s: torch.randn(*s, dtype=torch.bfloat16) * 0.05)(B, S, D)),
            "wqkv": _w((lambda *s: torch.randn(*s, dtype=torch.bfloat16) * 1.6)(D, 3 * D)),
            "wo": _w((lambda *s: torch.randn(*s, dtype=torch.bfloat16) * 0.1)(D, D)),
        }
    _A_in = _A_setup()

    def _B_setup():
        B, S, D = 1, 2048, 1024
        ks = 4
        return {
            "q": _w((lambda *s: torch.randn(*s, dtype=torch.bfloat16) * 0.05)(B, S, D)),
            "k": _w((lambda *s: torch.randn(*s, dtype=torch.bfloat16) * 0.8)(B, S, D)),
            "v": _w((lambda *s: torch.randn(*s, dtype=torch.bfloat16) * 0.4)(B, S, D)),
            "shift_k": _w((lambda *s: torch.randn(*s, dtype=torch.bfloat16) * 0.8)(D, ks) * 0.5),
            "ssm_k": _w((lambda *s: torch.randn(*s, dtype=torch.bfloat16) * 3.2)(D, S) * (1.0 / S ** 0.5)),
            "ssm_D": _w((lambda *s: torch.randn(*s, dtype=torch.bfloat16) * 0.8)(D)),
        }
    _B_in = _B_setup()

    return {
        "A_x": _A_in["x"],
        "A_wqkv": _A_in["wqkv"],
        "A_wo": _A_in["wo"],
        "B_q": _B_in["q"],
        "B_k": _B_in["k"],
        "B_v": _B_in["v"],
        "B_shift_k": _B_in["shift_k"],
        "B_ssm_k": _B_in["ssm_k"],
        "B_ssm_D": _B_in["ssm_D"],
    }

def reference(A_x, A_wqkv, A_wo, B_q, B_k, B_v, B_shift_k, B_ssm_k, B_ssm_D):
    def _A_ref(x, wqkv, wo):
        B, S, D = x.shape
        H = 32; Dh = D // H
        qkv = (lambda x, *d: x.permute(*d))((x @ wqkv).reshape(B, S, 3, H, Dh), 2, 0, 3, 1, 4)
        q, k, v = qkv[0], qkv[1], qkv[2]
        phi = lambda t: torch.where(t > 0, t + 1, torch.exp(t)) + 1e-6
        pq, pk = phi(q), phi(k)
        kv = torch.einsum("bhsd,bhse->bhde", pk, v)
        z = torch.einsum("bhsd->bhd", pk)
        num = torch.einsum("bhqd,bhde->bhqe", pq, kv)
        den = torch.einsum("bhqd,bhd->bhq", pq, z)[..., None] + 1e-6
        o = num / den
        return (lambda x, *d: x.permute(*d))(o, 0, 2, 1, 3).reshape(B, S, D) @ wo

    def _B_ref(q, k, v, shift_k, ssm_k, ssm_D):
        B, S, D = q.shape
        ks = shift_k.shape[-1]
        k_pad = _pad(k, ((0, 0), (ks - 1, 0), (0, 0)))
        k_shift = torch.zeros_like(k)
        for t in range(ks):
            k_shift = k_shift + k_pad[:, t:t + S, :] * shift_k[:, ks - 1 - t][None, None, :]
        kv = k_shift * v
        L = 2 * S
        kv_pad = _pad(kv, ((0, 0), (0, S), (0, 0)))
        kf_pad = _pad(ssm_k, ((0, 0), (0, S)))
        kv_f = (lambda x, axes=(-2,-1): torch.fft.rfft2(x.float(), dim=axes))(kv_pad[:, None, :, :], axes=(1, 2))
        kf_f = (lambda x, axes=(-2,-1): torch.fft.rfft2(x.float(), dim=axes))(kf_pad[:, None, :], axes=(1, 2))
        kf_f = (lambda x, *d: x.permute(*d))(kf_f, 1, 2, 0)
        y_f = kv_f * kf_f[None, :, :, :]
        y = (lambda x, s, axes=(-2,-1): torch.fft.irfft2(x, s=s, dim=axes).to(torch.bfloat16))(y_f, (1, L), axes=(1, 2))[:, 0, :S, :]
        y = y + kv * ssm_D[None, None, :]
        return q * y

    a = _A_ref(A_x, A_wqkv, A_wo)
    b = _B_ref(B_q, B_k, B_v, B_shift_k, B_ssm_k, B_ssm_D)
    return torch.concatenate([a, b], axis=-1)
```

```cpp
#include <hip/hip_runtime.h>
#include <stdint.h>

typedef unsigned short u16;
typedef __attribute__((ext_vector_type(8))) short bf16x8;
typedef __attribute__((ext_vector_type(4))) short s16x4;
typedef __attribute__((ext_vector_type(4))) float f32x4;
typedef __attribute__((ext_vector_type(16))) float f32x16;
typedef __attribute__((ext_vector_type(4))) unsigned u32x4;
typedef __attribute__((ext_vector_type(2))) unsigned u32x2;
typedef __attribute__((address_space(3))) s16x4* lds_s16x4_ptr;

#define DI __device__ __forceinline__

DI float bf2f(u16 v) { return __uint_as_float(((unsigned)v) << 16); }
DI u16 f2bf(float f) { __bf16 b = (__bf16)f; return __builtin_bit_cast(u16, b); }
DI float rbf(float f) { return bf2f(f2bf(f)); }

DI s16x4 tr_read(const u16* p) {
  return __builtin_amdgcn_ds_read_tr16_b64_v4i16((lds_s16x4_ptr)(p));
}

constexpr int S_ = 2048, D_ = 1024;

template <int BM, int BN, int WM, int WN, int MODE>
__global__ __launch_bounds__(WM * WN * 64) void gemm_k(const u16* __restrict__ A, int lda,
                                                       const u16* __restrict__ B, int ldb,
                                                       u16* __restrict__ C, int ldc, int K) {
  constexpr int T = WM * WN * 64;
  constexpr int BK = 64;
  constexpr int AS = BK + 8;
  constexpr int BS = BN + 8;
  constexpr int ACH = BM * 8 / T;
  constexpr int BCH = BK * (BN / 8) / T;
  constexpr int WTM = BM / WM, WTN = BN / WN;
  constexpr int MT = WTM / 16, NT = WTN / 16;
  constexpr int STAGE = BM * AS + BK * BS;
  __shared__ __attribute__((aligned(16))) u16 lds[2 * STAGE];

  const int tid = threadIdx.x, lane = tid & 63, wid = tid >> 6;
  const int wm = wid / WN, wn = wid % WN;
  const int bid = blockIdx.x;
  const int xcd = bid & 7, j = bid >> 3;
  const int bm = (xcd >> 1) * 4 + (j & 3);
  const int bn = (xcd & 1) * 8 + (j >> 2);
  const int brow = bm * BM, bcol = bn * BN;

  u32x4 ra[ACH], rb[BCH];
  auto gload = [&](int kt) {
#pragma unroll
    for (int i = 0; i < ACH; ++i) {
      int c = tid + i * T, r = c >> 3, cc = c & 7;
      ra[i] = *(const u32x4*)(A + (size_t)(brow + r) * lda + kt * BK + cc * 8);
    }
#pragma unroll
    for (int i = 0; i < BCH; ++i) {
      int c = tid + i * T, r = c / (BN / 8), cc = c % (BN / 8);
      rb[i] = *(const u32x4*)(B + (size_t)(kt * BK + r) * ldb + bcol + cc * 8);
    }
  };
  auto lwrite = [&](int buf) {
    u16* sA = lds + buf * STAGE;
    u16* sB = sA + BM * AS;
#pragma unroll
    for (int i = 0; i < ACH; ++i) {
      int c = tid + i * T, r = c >> 3, cc = c & 7;
      *(u32x4*)(sA + r * AS + cc * 8) = ra[i];
    }
#pragma unroll
    for (int i = 0; i < BCH; ++i) {
      int c = tid + i * T, r = c / (BN / 8), cc = c % (BN / 8);
      *(u32x4*)(sB + r * BS + cc * 8) = rb[i];
    }
  };

  f32x4 acc[MT][NT];
#pragma unroll
  for (int m = 0; m < MT; ++m)
#pragma unroll
    for (int n = 0; n < NT; ++n) acc[m][n] = (f32x4){0.f, 0.f, 0.f, 0.f};

  const int nt = K / BK;
  gload(0);
  lwrite(0);
  __syncthreads();
  const int g = lane >> 4, q = (lane & 15) >> 2, p = lane & 3;
  for (int kt = 0; kt < nt; ++kt) {
    if (kt + 1 < nt) gload(kt + 1);
    const u16* sA = lds + (kt & 1) * STAGE;
    const u16* sB = sA + BM * AS;
#pragma unroll
    for (int ks = 0; ks < 2; ++ks) {
      bf16x8 af[MT];
#pragma unroll
      for (int m = 0; m < MT; ++m)
        af[m] = *(const bf16x8*)(sA + (wm * WTM + m * 16 + (lane & 15)) * AS + ks * 32 + g * 8);
      bf16x8 bfr[NT];
#pragma unroll
      for (int n = 0; n < NT; ++n) {
        const u16* pb = sB + (ks * 32 + 8 * g + q) * BS + wn * WTN + n * 16 + 4 * p;
        s16x4 lo = tr_read(pb);
        s16x4 hi = tr_read(pb + 4 * BS);
        bfr[n] = __builtin_shufflevector(lo, hi, 0, 1, 2, 3, 4, 5, 6, 7);
      }
#pragma unroll
      for (int m = 0; m < MT; ++m)
#pragma unroll
        for (int n = 0; n < NT; ++n)
          acc[m][n] = __builtin_amdgcn_mfma_f32_16x16x32_bf16(af[m], bfr[n], acc[m][n], 0, 0, 0);
    }
    if (kt + 1 < nt) lwrite((kt + 1) & 1);
    __syncthreads();
  }
  constexpr int CS = BN + 8;
  u16* sC = lds;
#pragma unroll
  for (int m = 0; m < MT; ++m)
#pragma unroll
    for (int n = 0; n < NT; ++n)
#pragma unroll
      for (int jj = 0; jj < 4; ++jj) {
        int r = wm * WTM + m * 16 + g * 4 + jj;
        int c = wn * WTN + n * 16 + (lane & 15);
        float t = rbf(acc[m][n][jj]);
        if (MODE == 1) {
          if (bcol + c < 2048) {
            float r1 = (t > 0.f) ? rbf(t + 1.0f) : rbf(expf(t));
            t = rbf(r1 + 1e-6f);
          }
        }
        sC[r * CS + c] = f2bf(t);
      }
  __syncthreads();
  for (int c = tid; c < BM * (BN / 8); c += T) {
    int r = c / (BN / 8), cc = c % (BN / 8);
    *(u32x4*)(C + (size_t)(brow + r) * ldc + bcol + cc * 8) = *(const u32x4*)(sC + r * CS + cc * 8);
  }
}

__global__ __launch_bounds__(256) void kvpart_k(const u16* __restrict__ qkv, float* __restrict__ part,
                                                float* __restrict__ zpart) {
  __shared__ __attribute__((aligned(16))) u16 spk[256 * 40];
  __shared__ __attribute__((aligned(16))) u16 sv[256 * 40];
  const int h = blockIdx.x & 31, sp = blockIdx.x >> 5, t = threadIdx.x;
  const u16* rowp = qkv + (size_t)(sp * 256 + t) * 3072;
#pragma unroll
  for (int i = 0; i < 4; ++i) {
    *(u32x4*)(spk + t * 40 + i * 8) = *(const u32x4*)(rowp + 1024 + h * 32 + i * 8);
    *(u32x4*)(sv + t * 40 + i * 8) = *(const u32x4*)(rowp + 2048 + h * 32 + i * 8);
  }
  __syncthreads();
  const int d = t >> 3, e0 = (t & 7) * 4;
  float a0 = 0, a1 = 0, a2 = 0, a3 = 0, zz = 0;
  for (int s = 0; s < 256; ++s) {
    float a = bf2f(spk[s * 40 + d]);
    u32x2 vv = *(const u32x2*)(sv + s * 40 + e0);
    a0 += a * __uint_as_float(vv[0] << 16);
    a1 += a * __uint_as_float(vv[0] & 0xffff0000u);
    a2 += a * __uint_as_float(vv[1] << 16);
    a3 += a * __uint_as_float(vv[1] & 0xffff0000u);
    zz += a;
  }
  float* pp = part + ((size_t)(sp * 32 + h) * 32 + d) * 32 + e0;
  *(f32x4*)pp = (f32x4){a0, a1, a2, a3};
  if ((t & 7) == 0) zpart[(sp * 32 + h) * 32 + d] = zz;
}

__global__ __launch_bounds__(256) void o_k(const u16* __restrict__ qkv, const float* __restrict__ part,
                                           const float* __restrict__ zpart, u16* __restrict__ o) {
  __shared__ float skv[4 * 32 * 33];
  __shared__ float sz[4 * 32];
  __shared__ __attribute__((aligned(16))) u16 spq[64 * 136];
  const int rb = blockIdx.x >> 3, hg = blockIdx.x & 7, t = threadIdx.x;
  for (int i = t; i < 4 * 1024; i += 256) {
    int hh = i >> 10, de = i & 1023;
    float s = 0;
#pragma unroll
    for (int sp = 0; sp < 8; ++sp) s += part[(size_t)(sp * 32 + hg * 4 + hh) * 1024 + de];
    skv[hh * 1056 + (de >> 5) * 33 + (de & 31)] = rbf(s);
  }
  if (t < 128) {
    int hh = t >> 5, d = t & 31;
    float s = 0;
#pragma unroll
    for (int sp = 0; sp < 8; ++sp) s += zpart[(sp * 32 + hg * 4 + hh) * 32 + d];
    sz[t] = rbf(s);
  }
  for (int c = t; c < 64 * 16; c += 256) {
    int r = c >> 4, cc = c & 15;
    *(u32x4*)(spq + r * 136 + cc * 8) = *(const u32x4*)(qkv + (size_t)(rb * 64 + r) * 3072 + hg * 128 + cc * 8);
  }
  __syncthreads();
  const int r = t >> 2, hh = t & 3;
  float pq[32];
#pragma unroll
  for (int d = 0; d < 32; ++d) pq[d] = bf2f(spq[r * 136 + hh * 32 + d]);
  float den = 0;
#pragma unroll
  for (int d = 0; d < 32; ++d) den += pq[d] * sz[hh * 32 + d];
  den = rbf(rbf(den) + 1e-6f);
  u16 ov[32];
#pragma unroll
  for (int e = 0; e < 32; ++e) {
    float num = 0;
#pragma unroll
    for (int d = 0; d < 32; ++d) num += pq[d] * skv[hh * 1056 + d * 33 + e];
    ov[e] = f2bf(rbf(num) / den);
  }
  u16* op = o + (size_t)(rb * 64 + r) * 1024 + hg * 128 + hh * 32;
#pragma unroll
  for (int i = 0; i < 4; ++i) {
    u32x4 w;
#pragma unroll
    for (int k = 0; k < 4; ++k) w[k] = (unsigned)ov[i * 8 + 2 * k] | ((unsigned)ov[i * 8 + 2 * k + 1] << 16);
    *(u32x4*)(op + i * 8) = w;
  }
}

__global__ __launch_bounds__(256) void pre_k(const u16* __restrict__ k, const u16* __restrict__ v,
                                             const u16* __restrict__ shk, u16* __restrict__ uT) {
  __shared__ __attribute__((aligned(16))) u16 tile[64 * 72];
  const int sb = blockIdx.x >> 4, db = blockIdx.x & 15, t = threadIdx.x;
  const int cc = t & 7, d0 = db * 64 + cc * 8;
  float sh[8][4];
#pragma unroll
  for (int i = 0; i < 8; ++i)
#pragma unroll
    for (int j = 0; j < 4; ++j) sh[i][j] = bf2f(shk[(d0 + i) * 4 + j]);
#pragma unroll
  for (int rr = 0; rr < 2; ++rr) {
    int r = (t >> 3) + rr * 32, s = sb * 64 + r;
    float acc[8];
#pragma unroll
    for (int i = 0; i < 8; ++i) acc[i] = 0.f;
#pragma unroll
    for (int tt = 0; tt < 4; ++tt) {
      int ss = s + tt - 3;
      if (ss >= 0) {
        u32x4 kk = *(const u32x4*)(k + (size_t)ss * D_ + d0);
#pragma unroll
        for (int i = 0; i < 8; ++i) {
          float kvv = (i & 1) ? __uint_as_float(kk[i >> 1] & 0xffff0000u) : __uint_as_float(kk[i >> 1] << 16);
          acc[i] = rbf(acc[i] + rbf(kvv * sh[i][3 - tt]));
        }
      }
    }
    u32x4 vv = *(const u32x4*)(v + (size_t)s * D_ + d0);
#pragma unroll
    for (int i = 0; i < 8; ++i) {
      float vf = (i & 1) ? __uint_as_float(vv[i >> 1] & 0xffff0000u) : __uint_as_float(vv[i >> 1] << 16);
      tile[(cc * 8 + i) * 72 + r] = f2bf(acc[i] * vf);
    }
  }
  __syncthreads();
  for (int c = t; c < 64 * 8; c += 256) {
    int d = c >> 3, c8 = c & 7;
    *(u32x4*)(uT + (size_t)(db * 64 + d) * S_ + sb * 64 + c8 * 8) = *(const u32x4*)(tile + d * 72 + c8 * 8);
  }
}

constexpr int GS = 2120;
__global__ __launch_bounds__(256) void conv_k(const u16* __restrict__ uT, const u16* __restrict__ hk,
                                              u16* __restrict__ yT) {
  __shared__ __attribute__((aligned(16))) u16 G[8 * GS];
  __shared__ __attribute__((aligned(16))) u16 U[64 * 72];
  __shared__ float red[4 * 2048];
  const int d = blockIdx.x, t = threadIdx.x, lane = t & 63, w = t >> 6;
  const u16* h = hk + (size_t)d * S_;
  for (int idx = t; idx < 8 * 2112; idx += 256) {
    int s = idx / 2112, y = idx - s * 2112, x = y + s;
    G[s * GS + y] = (x <= 2047) ? h[2047 - x] : (u16)0;
  }
  for (int idx = t; idx < 64 * 9; idx += 256) {
    int b = idx / 9, c8 = idx - b * 9;
    u32x4 val = (u32x4){0u, 0u, 0u, 0u};
    if (b >= 32 && c8 < 8) val = *(const u32x4*)(uT + (size_t)d * S_ + (b - 32) * 64 + c8 * 8);
    *(u32x4*)(U + b * 72 + c8 * 8) = val;
  }
  __syncthreads();
  f32x16 acc0, acc1;
#pragma unroll
  for (int i = 0; i < 16; ++i) { acc0[i] = 0.f; acc1[i] = 0.f; }
  const int n = lane & 31, hh = lane >> 5;
  for (int m = w; m < 32; m += 4) {
#pragma unroll
    for (int kk = 0; kk < 4; ++kk) {
      bf16x8 bfr = *(const bf16x8*)(U + (n - m + 32) * 72 + kk * 16 + 8 * hh);
      int x0 = 2047 - 64 * m - n + kk * 16 + 8 * hh;
      int s0 = x0 & 7;
      bf16x8 a0 = *(const bf16x8*)(G + s0 * GS + (x0 - s0));
      int x1 = x0 - 32;
      bf16x8 a1 = *(const bf16x8*)(G + s0 * GS + (x1 - s0));
      acc0 = __builtin_amdgcn_mfma_f32_32x32x16_bf16(a0, bfr, acc0, 0, 0, 0);
      acc1 = __builtin_amdgcn_mfma_f32_32x32x16_bf16(a1, bfr, acc1, 0, 0, 0);
    }
  }
#pragma unroll
  for (int i = 0; i < 16; ++i) {
    int row = (i & 3) + 8 * (i >> 2) + 4 * hh;
    red[w * 2048 + n * 64 + row] = acc0[i];
    red[w * 2048 + n * 64 + 32 + row] = acc1[i];
  }
  __syncthreads();
  for (int idx = t; idx < 2048; idx += 256) {
    float s = red[idx] + red[2048 + idx] + red[4096 + idx] + red[6144 + idx];
    yT[(size_t)d * S_ + idx] = f2bf(s);
  }
}

__global__ __launch_bounds__(256) void post_k(const u16* __restrict__ uT, const u16* __restrict__ yT,
                                              const u16* __restrict__ q, const u16* __restrict__ Dv,
                                              u16* __restrict__ out) {
  __shared__ __attribute__((aligned(16))) u16 tu[64 * 72];
  __shared__ __attribute__((aligned(16))) u16 ty[64 * 72];
  const int sb = blockIdx.x >> 4, db = blockIdx.x & 15, t = threadIdx.x;
  for (int c = t; c < 64 * 8; c += 256) {
    int d = c >> 3, c8 = c & 7;
    *(u32x4*)(tu + d * 72 + c8 * 8) = *(const u32x4*)(uT + (size_t)(db * 64 + d) * S_ + sb * 64 + c8 * 8);
    *(u32x4*)(ty + d * 72 + c8 * 8) = *(const u32x4*)(yT + (size_t)(db * 64 + d) * S_ + sb * 64 + c8 * 8);
  }
  __syncthreads();
  const int cc = t & 7, d0 = db * 64 + cc * 8;
#pragma unroll
  for (int rr = 0; rr < 2; ++rr) {
    int r = (t >> 3) + rr * 32, s = sb * 64 + r;
    u32x4 qq = *(const u32x4*)(q + (size_t)s * D_ + d0);
    u16 ov[8];
#pragma unroll
    for (int i = 0; i < 8; ++i) {
      float qf = (i & 1) ? __uint_as_float(qq[i >> 1] & 0xffff0000u) : __uint_as_float(qq[i >> 1] << 16);
      float kvv = bf2f(tu[(cc * 8 + i) * 72 + r]);
      float y = bf2f(ty[(cc * 8 + i) * 72 + r]);
      float y2 = rbf(y + rbf(kvv * bf2f(Dv[d0 + i])));
      ov[i] = f2bf(qf * y2);
    }
    u32x4 w;
#pragma unroll
    for (int k = 0; k < 4; ++k) w[k] = (unsigned)ov[2 * k] | ((unsigned)ov[2 * k + 1] << 16);
    *(u32x4*)(out + (size_t)s * 2048 + 1024 + d0) = w;
  }
}

extern "C" void kernel_launch(void* const* d_in, const int* in_sizes, int n_in, void* d_out, int out_size,
                              void* d_ws, size_t ws_size, hipStream_t stream) {
  const u16* x = (const u16*)d_in[0];
  const u16* wqkv = (const u16*)d_in[1];
  const u16* wo = (const u16*)d_in[2];
  const u16* bq = (const u16*)d_in[3];
  const u16* bk = (const u16*)d_in[4];
  const u16* bv = (const u16*)d_in[5];
  const u16* shk = (const u16*)d_in[6];
  const u16* ssk = (const u16*)d_in[7];
  const u16* ssD = (const u16*)d_in[8];
  u16* out = (u16*)d_out;
  char* ws = (char*)d_ws;
  u16* qkv = (u16*)(ws);
  float* part = (float*)(ws + (12u << 20));
  float* zpart = (float*)(ws + (13u << 20));
  u16* o = (u16*)(ws + (14u << 20));
  u16* uT = (u16*)(ws + (18u << 20));
  u16* yT = (u16*)(ws + (22u << 20));

  gemm_k<128, 192, 2, 4, 1><<<256, 512, 0, stream>>>(x, 1024, wqkv, 3072, qkv, 3072, 1024);
  pre_k<<<512, 256, 0, stream>>>(bk, bv, shk, uT);
  kvpart_k<<<256, 256, 0, stream>>>(qkv, part, zpart);
  conv_k<<<1024, 256, 0, stream>>>(uT, ssk, yT);
  o_k<<<256, 256, 0, stream>>>(qkv, part, zpart, o);
  post_k<<<512, 256, 0, stream>>>(uT, yT, bq, ssD, out);
  gemm_k<128, 64, 2, 2, 0><<<256, 256, 0, stream>>>(o, 1024, wo, 1024, out, 2048, 1024);
}
```

```cpp
#include <hip/hip_runtime.h>
#include <stdint.h>

typedef unsigned short u16;
typedef __attribute__((ext_vector_type(8))) short bf16x8;
typedef __attribute__((ext_vector_type(4))) short s16x4;
typedef __attribute__((ext_vector_type(4))) float f32x4;
typedef __attribute__((ext_vector_type(16))) float f32x16;
typedef __attribute__((ext_vector_type(4))) unsigned u32x4;
typedef __attribute__((ext_vector_type(2))) unsigned u32x2;
typedef __attribute__((address_space(3))) s16x4* lds_s16x4_ptr;

#define DI __device__ __forceinline__

DI float bf2f(u16 v) { return __uint_as_float(((unsigned)v) << 16); }
DI u16 f2bf(float f) { __bf16 b = (__bf16)f; return __builtin_bit_cast(u16, b); }
DI float rbf(float f) { return bf2f(f2bf(f)); }

DI s16x4 tr_read(const u16* p) {
  return __builtin_amdgcn_ds_read_tr16_b64_v4i16((lds_s16x4_ptr)(p));
}

constexpr int S_ = 2048, D_ = 1024;

template <int BM, int BN, int WM, int WN, int MODE>
__global__ __launch_bounds__(WM * WN * 64) void gemm_k(const u16* __restrict__ A, int lda,
                                                       const u16* __restrict__ B, int ldb,
                                                       u16* __restrict__ C, int ldc, int K) {
  constexpr int T = WM * WN * 64;
  constexpr int BK = 64;
  constexpr int AS = BK + 8;
  constexpr int BS = BN + 8;
  constexpr int ACH = BM * 8 / T;
  constexpr int BCH = BK * (BN / 8) / T;
  constexpr int WTM = BM / WM, WTN = BN / WN;
  constexpr int MT = WTM / 16, NT = WTN / 16;
  constexpr int STAGE = BM * AS + BK * BS;
  __shared__ __attribute__((aligned(16))) u16 lds[2 * STAGE];

  const int tid = threadIdx.x, lane = tid & 63, wid = tid >> 6;
  const int wm = wid / WN, wn = wid % WN;
  const int bid = blockIdx.x;
  const int xcd = bid & 7, j = bid >> 3;
  const int bm = (xcd >> 1) * 4 + (j & 3);
  const int bn = (xcd & 1) * 8 + (j >> 2);
  const int brow = bm * BM, bcol = bn * BN;

  u32x4 ra[ACH], rb[BCH];
  auto gload = [&](int kt) {
#pragma unroll
    for (int i = 0; i < ACH; ++i) {
      int c = tid + i * T, r = c >> 3, cc = c & 7;
      ra[i] = *(const u32x4*)(A + (size_t)(brow + r) * lda + kt * BK + cc * 8);
    }
#pragma unroll
    for (int i = 0; i < BCH; ++i) {
      int c = tid + i * T, r = c / (BN / 8), cc = c % (BN / 8);
      rb[i] = *(const u32x4*)(B + (size_t)(kt * BK + r) * ldb + bcol + cc * 8);
    }
  };
  auto lwrite = [&](int buf) {
    u16* sA = lds + buf * STAGE;
    u16* sB = sA + BM * AS;
#pragma unroll
    for (int i = 0; i < ACH; ++i) {
      int c = tid + i * T, r = c >> 3, cc = c & 7;
      *(u32x4*)(sA + r * AS + cc * 8) = ra[i];
    }
#pragma unroll
    for (int i = 0; i < BCH; ++i) {
      int c = tid + i * T, r = c / (BN / 8), cc = c % (BN / 8);
      *(u32x4*)(sB + r * BS + cc * 8) = rb[i];
    }
  };

  f32x4 acc[MT][NT];
#pragma unroll
  for (int m = 0; m < MT; ++m)
#pragma unroll
    for (int n = 0; n < NT; ++n) acc[m][n] = (f32x4){0.f, 0.f, 0.f, 0.f};

  const int nt = K / BK;
  gload(0);
  lwrite(0);
  __syncthreads();
  const int g = lane >> 4, q = (lane & 15) >> 2, p = lane & 3;
  for (int kt = 0; kt < nt; ++kt) {
    if (kt + 1 < nt) gload(kt + 1);
    const u16* sA = lds + (kt & 1) * STAGE;
    const u16* sB = sA + BM * AS;
#pragma unroll
    for (int ks = 0; ks < 2; ++ks) {
      bf16x8 af[MT];
#pragma unroll
      for (int m = 0; m < MT; ++m)
        af[m] = *(const bf16x8*)(sA + (wm * WTM + m * 16 + (lane & 15)) * AS + ks * 32 + g * 8);
      bf16x8 bfr[NT];
#pragma unroll
      for (int n = 0; n < NT; ++n) {
        const u16* pb = sB + (ks * 32 + 8 * g + q) * BS + wn * WTN + n * 16 + 4 * p;
        s16x4 lo = tr_read(pb);
        s16x4 hi = tr_read(pb + 4 * BS);
        bfr[n] = __builtin_shufflevector(lo, hi, 0, 1, 2, 3, 4, 5, 6, 7);
      }
#pragma unroll
      for (int m = 0; m < MT; ++m)
#pragma unroll
        for (int n = 0; n < NT; ++n)
          acc[m][n] = __builtin_amdgcn_mfma_f32_16x16x32_bf16(af[m], bfr[n], acc[m][n], 0, 0, 0);
    }
    if (kt + 1 < nt) lwrite((kt + 1) & 1);
    __syncthreads();
  }
  constexpr int CS = BN + 8;
  u16* sC = lds;
#pragma unroll
  for (int m = 0; m < MT; ++m)
#pragma unroll
    for (int n = 0; n < NT; ++n)
#pragma unroll
      for (int jj = 0; jj < 4; ++jj) {
        int r = wm * WTM + m * 16 + g * 4 + jj;
        int c = wn * WTN + n * 16 + (lane & 15);
        float t = rbf(acc[m][n][jj]);
        if (MODE == 1) {
          if (bcol + c < 2048) {
            float r1 = (t > 0.f) ? rbf(t + 1.0f) : rbf(expf(t));
            t = rbf(r1 + 1e-6f);
          }
        }
        sC[r * CS + c] = f2bf(t);
      }
  __syncthreads();
  for (int c = tid; c < BM * (BN / 8); c += T) {
    int r = c / (BN / 8), cc = c % (BN / 8);
    *(u32x4*)(C + (size_t)(brow + r) * ldc + bcol + cc * 8) = *(const u32x4*)(sC + r * CS + cc * 8);
  }
}

DI bf16x8 trfrag32(const u16* base, int stride, int ks, int lane) {
  const int h = lane >> 5, blk = (lane >> 4) & 1, q = (lane & 15) >> 2, p = lane & 3;
  const u16* pa = base + (16 * ks + 8 * h + q) * stride + 16 * blk + 4 * p;
  s16x4 lo = tr_read(pa);
  s16x4 hi = tr_read(pa + 4 * stride);
  return __builtin_shufflevector(lo, hi, 0, 1, 2, 3, 4, 5, 6, 7);
}

__global__ __launch_bounds__(256) void kvpart_k(const u16* __restrict__ qkv, float* __restrict__ part,
                                                float* __restrict__ zpart) {
  __shared__ __attribute__((aligned(16))) u16 spk[256 * 40];
  __shared__ __attribute__((aligned(16))) u16 sv[256 * 40];
  __shared__ float red[4 * 1056];
  const int h = blockIdx.x & 31, sp = blockIdx.x >> 5, t = threadIdx.x, lane = t & 63, w = t >> 6;
#pragma unroll
  for (int i = 0; i < 4; ++i) {
    int c = t + i * 256, r = c >> 2, cc = c & 3;
    const u16* rowp = qkv + (size_t)(sp * 256 + r) * 3072;
    *(u32x4*)(spk + r * 40 + cc * 8) = *(const u32x4*)(rowp + 1024 + h * 32 + cc * 8);
    *(u32x4*)(sv + r * 40 + cc * 8) = *(const u32x4*)(rowp + 2048 + h * 32 + cc * 8);
  }
  __syncthreads();
  f32x16 akv, az;
#pragma unroll
  for (int i = 0; i < 16; ++i) { akv[i] = 0.f; az[i] = 0.f; }
  bf16x8 ones;
#pragma unroll
  for (int i = 0; i < 8; ++i) ones[i] = (short)0x3f80;
#pragma unroll
  for (int ks = 0; ks < 4; ++ks) {
    bf16x8 a = trfrag32(spk + w * 64 * 40, 40, ks, lane);
    bf16x8 b = trfrag32(sv + w * 64 * 40, 40, ks, lane);
    akv = __builtin_amdgcn_mfma_f32_32x32x16_bf16(a, b, akv, 0, 0, 0);
    az = __builtin_amdgcn_mfma_f32_32x32x16_bf16(a, ones, az, 0, 0, 0);
  }
  const int n = lane & 31, hh = lane >> 5;
#pragma unroll
  for (int i = 0; i < 16; ++i) {
    int row = (i & 3) + 8 * (i >> 2) + 4 * hh;
    red[w * 1056 + row * 32 + n] = akv[i];
    if (n == 0) red[w * 1056 + 1024 + row] = az[i];
  }
  __syncthreads();
  for (int i = t; i < 1056; i += 256) {
    float s = red[i] + red[1056 + i] + red[2112 + i] + red[3168 + i];
    if (i < 1024) part[(size_t)(sp * 32 + h) * 1024 + i] = s;
    else zpart[(sp * 32 + h) * 32 + (i - 1024)] = s;
  }
}

__global__ __launch_bounds__(256) void o_k(const u16* __restrict__ qkv, const float* __restrict__ part,
                                           const float* __restrict__ zpart, u16* __restrict__ o) {
  __shared__ __attribute__((aligned(16))) u16 skvT[2 * 32 * 40];
  __shared__ __attribute__((aligned(16))) u16 sz[2 * 32];
  const int rb = blockIdx.x >> 4, hg = blockIdx.x & 15, t = threadIdx.x, lane = t & 63, w = t >> 6;
  for (int i = t; i < 2 * 1024; i += 256) {
    int hh = i >> 10, de = i & 1023;
    float s = 0;
#pragma unroll
    for (int sp = 0; sp < 8; ++sp) s += part[(size_t)(sp * 32 + hg * 2 + hh) * 1024 + de];
    skvT[hh * 1280 + (de & 31) * 40 + (de >> 5)] = f2bf(s);
  }
  if (t < 64) {
    int hh = t >> 5, d = t & 31;
    float s = 0;
#pragma unroll
    for (int sp = 0; sp < 8; ++sp) s += zpart[(sp * 32 + hg * 2 + hh) * 32 + d];
    sz[t] = f2bf(s);
  }
  __syncthreads();
  const int n = lane & 31, hf = lane >> 5;
  const int row0 = rb * 128 + w * 32;
#pragma unroll
  for (int hh = 0; hh < 2; ++hh) {
    f32x16 an, ad;
#pragma unroll
    for (int i = 0; i < 16; ++i) { an[i] = 0.f; ad[i] = 0.f; }
#pragma unroll
    for (int ks = 0; ks < 2; ++ks) {
      bf16x8 a = *(const bf16x8*)(qkv + (size_t)(row0 + n) * 3072 + (hg * 2 + hh) * 32 + ks * 16 + 8 * hf);
      bf16x8 b = *(const bf16x8*)(skvT + hh * 1280 + n * 40 + ks * 16 + 8 * hf);
      bf16x8 bz = *(const bf16x8*)(sz + hh * 32 + ks * 16 + 8 * hf);
      an = __builtin_amdgcn_mfma_f32_32x32x16_bf16(a, b, an, 0, 0, 0);
      ad = __builtin_amdgcn_mfma_f32_32x32x16_bf16(a, bz, ad, 0, 0, 0);
    }
#pragma unroll
    for (int i = 0; i < 16; ++i) {
      int row = (i & 3) + 8 * (i >> 2) + 4 * hf;
      float den = rbf(rbf(ad[i]) + 1e-6f);
      o[(size_t)(row0 + row) * 1024 + (hg * 2 + hh) * 32 + n] = f2bf(rbf(an[i]) / den);
    }
  }
}

__global__ __launch_bounds__(256) void pre_k(const u16* __restrict__ k, const u16* __restrict__ v,
                                             const u16* __restrict__ shk, u16* __restrict__ uT) {
  __shared__ __attribute__((aligned(16))) u16 tile[64 * 72];
  const int sb = blockIdx.x >> 4, db = blockIdx.x & 15, t = threadIdx.x;
  const int cc = t & 7, d0 = db * 64 + cc * 8;
  float sh[8][4];
#pragma unroll
  for (int i = 0; i < 8; ++i)
#pragma unroll
    for (int j = 0; j < 4; ++j) sh[i][j] = bf2f(shk[(d0 + i) * 4 + j]);
#pragma unroll
  for (int rr = 0; rr < 2; ++rr) {
    int r = (t >> 3) + rr * 32, s = sb * 64 + r;
    float acc[8];
#pragma unroll
    for (int i = 0; i < 8; ++i) acc[i] = 0.f;
#pragma unroll
    for (int tt = 0; tt < 4; ++tt) {
      int ss = s + tt - 3;
      if (ss >= 0) {
        u32x4 kk = *(const u32x4*)(k + (size_t)ss * D_ + d0);
#pragma unroll
        for (int i = 0; i < 8; ++i) {
          float kvv = (i & 1) ? __uint_as_float(kk[i >> 1] & 0xffff0000u) : __uint_as_float(kk[i >> 1] << 16);
          acc[i] = rbf(acc[i] + rbf(kvv * sh[i][3 - tt]));
        }
      }
    }
    u32x4 vv = *(const u32x4*)(v + (size_t)s * D_ + d0);
#pragma unroll
    for (int i = 0; i < 8; ++i) {
      float vf = (i & 1) ? __uint_as_float(vv[i >> 1] & 0xffff0000u) : __uint_as_float(vv[i >> 1] << 16);
      tile[(cc * 8 + i) * 72 + r] = f2bf(acc[i] * vf);
    }
  }
  __syncthreads();
  for (int c = t; c < 64 * 8; c += 256) {
    int d = c >> 3, c8 = c & 7;
    *(u32x4*)(uT + (size_t)(db * 64 + d) * S_ + sb * 64 + c8 * 8) = *(const u32x4*)(tile + d * 72 + c8 * 8);
  }
}

constexpr int WS = 328;
constexpr int HS = 2112;
constexpr int CW = 8 * WS + 64 * 72 + HS;
__global__ __launch_bounds__(256) void conv_k(const u16* __restrict__ uT, const u16* __restrict__ hk,
                                              u16* __restrict__ yT) {
  __shared__ __attribute__((aligned(16))) u16 lds[4 * CW];
  const int t = threadIdx.x, lane = t & 63, w = t >> 6;
  const int d = blockIdx.x * 4 + w;
  u16* G = lds + w * CW;
  u16* U = G + 8 * WS;
  u16* HR = U + 64 * 72;
  const u16* h = hk + (size_t)d * S_;
#pragma unroll
  for (int i = 0; i < 4; ++i) {
    int c = lane + i * 64;
    u32x4 v = *(const u32x4*)(h + c * 8);
#pragma unroll
    for (int j = 0; j < 4; ++j) {
      HR[2047 - (c * 8 + 2 * j)] = (u16)(v[j] & 0xffff);
      HR[2047 - (c * 8 + 2 * j + 1)] = (u16)(v[j] >> 16);
    }
  }
  HR[2048 + lane] = 0;
  for (int idx = lane; idx < 64 * 9; idx += 64) {
    int b = idx / 9, c8 = idx - b * 9;
    u32x4 val = (u32x4){0u, 0u, 0u, 0u};
    if (b >= 32 && c8 < 8) val = *(const u32x4*)(uT + (size_t)d * S_ + (b - 32) * 64 + c8 * 8);
    *(u32x4*)(U + b * 72 + c8 * 8) = val;
  }
  __syncthreads();
  f32x16 acc0, acc1;
#pragma unroll
  for (int i = 0; i < 16; ++i) { acc0[i] = 0.f; acc1[i] = 0.f; }
  const int n = lane & 31, hh = lane >> 5;
  for (int m0 = 0; m0 < 32; m0 += 4) {
    const int xb = 1792 - 64 * m0;
#pragma unroll
    for (int i = 0; i < 5; ++i) {
      int y = lane + i * 64;
      u16 v = HR[xb + y];
#pragma unroll
      for (int s = 0; s < 8; ++s)
        if (y - s >= 0) G[s * WS + y - s] = v;
    }
    __syncthreads();
#pragma unroll
    for (int mm = 0; mm < 4; ++mm) {
      const int m = m0 + mm;
#pragma unroll
      for (int kk = 0; kk < 4; ++kk) {
        bf16x8 bfr = *(const bf16x8*)(U + (n - m + 32) * 72 + kk * 16 + 8 * hh);
        int x0 = 2047 - 64 * m - n + kk * 16 + 8 * hh;
        int s0 = x0 & 7;
        const u16* gp = G + s0 * WS + (x0 - s0 - xb);
        bf16x8 a0 = *(const bf16x8*)(gp);
        bf16x8 a1 = *(const bf16x8*)(gp - 32);
        acc0 = __builtin_amdgcn_mfma_f32_32x32x16_bf16(a0, bfr, acc0, 0, 0, 0);
        acc1 = __builtin_amdgcn_mfma_f32_32x32x16_bf16(a1, bfr, acc1, 0, 0, 0);
      }
    }
    __syncthreads();
  }
#pragma unroll
  for (int i = 0; i < 16; ++i) {
    int row = (i & 3) + 8 * (i >> 2) + 4 * hh;
    HR[n * 64 + row] = f2bf(acc0[i]);
    HR[n * 64 + 32 + row] = f2bf(acc1[i]);
  }
  __syncthreads();
#pragma unroll
  for (int i = 0; i < 4; ++i) {
    int c = lane + i * 64;
    *(u32x4*)(yT + (size_t)d * S_ + c * 8) = *(const u32x4*)(HR + c * 8);
  }
}

__global__ __launch_bounds__(256) void post_k(const u16* __restrict__ uT, const u16* __restrict__ yT,
                                              const u16* __restrict__ q, const u16* __restrict__ Dv,
                                              u16* __restrict__ out) {
  __shared__ __attribute__((aligned(16))) u16 tu[64 * 72];
  __shared__ __attribute__((aligned(16))) u16 ty[64 * 72];
  const int sb = blockIdx.x >> 4, db = blockIdx.x & 15, t = threadIdx.x;
  for (int c = t; c < 64 * 8; c += 256) {
    int d = c >> 3, c8 = c & 7;
    *(u32x4*)(tu + d * 72 + c8 * 8) = *(const u32x4*)(uT + (size_t)(db * 64 + d) * S_ + sb * 64 + c8 * 8);
    *(u32x4*)(ty + d * 72 + c8 * 8) = *(const u32x4*)(yT + (size_t)(db * 64 + d) * S_ + sb * 64 + c8 * 8);
  }
  __syncthreads();
  const int cc = t & 7, d0 = db * 64 + cc * 8;
#pragma unroll
  for (int rr = 0; rr < 2; ++rr) {
    int r = (t >> 3) + rr * 32, s = sb * 64 + r;
    u32x4 qq = *(const u32x4*)(q + (size_t)s * D_ + d0);
    u16 ov[8];
#pragma unroll
    for (int i = 0; i < 8; ++i) {
      float qf = (i & 1) ? __uint_as_float(qq[i >> 1] & 0xffff0000u) : __uint_as_float(qq[i >> 1] << 16);
      float kvv = bf2f(tu[(cc * 8 + i) * 72 + r]);
      float y = bf2f(ty[(cc * 8 + i) * 72 + r]);
      float y2 = rbf(y + rbf(kvv * bf2f(Dv[d0 + i])));
      ov[i] = f2bf(qf * y2);
    }
    u32x4 w;
#pragma unroll
    for (int k = 0; k < 4; ++k) w[k] = (unsigned)ov[2 * k] | ((unsigned)ov[2 * k + 1] << 16);
    *(u32x4*)(out + (size_t)s * 2048 + 1024 + d0) = w;
  }
}

extern "C" void kernel_launch(void* const* d_in, const int* in_sizes, int n_in, void* d_out, int out_size,
                              void* d_ws, size_t ws_size, hipStream_t stream) {
  const u16* x = (const u16*)d_in[0];
  const u16* wqkv = (const u16*)d_in[1];
  const u16* wo = (const u16*)d_in[2];
  const u16* bq = (const u16*)d_in[3];
  const u16* bk = (const u16*)d_in[4];
  const u16* bv = (const u16*)d_in[5];
  const u16* shk = (const u16*)d_in[6];
  const u16* ssk = (const u16*)d_in[7];
  const u16* ssD = (const u16*)d_in[8];
  u16* out = (u16*)d_out;
  char* ws = (char*)d_ws;
  u16* qkv = (u16*)(ws);
  float* part = (float*)(ws + (12u << 20));
  float* zpart = (float*)(ws + (13u << 20));
  u16* o = (u16*)(ws + (14u << 20));
  u16* uT = (u16*)(ws + (18u << 20));
  u16* yT = (u16*)(ws + (22u << 20));

  gemm_k<128, 192, 2, 4, 1><<<256, 512, 0, stream>>>(x, 1024, wqkv, 3072, qkv, 3072, 1024);
  pre_k<<<512, 256, 0, stream>>>(bk, bv, shk, uT);
  kvpart_k<<<256, 256, 0, stream>>>(qkv, part, zpart);
  conv_k<<<256, 256, 0, stream>>>(uT, ssk, yT);
  o_k<<<256, 256, 0, stream>>>(qkv, part, zpart, o);
  post_k<<<512, 256, 0, stream>>>(uT, yT, bq, ssD, out);
  gemm_k<128, 64, 2, 2, 0><<<256, 256, 0, stream>>>(o, 1024, wo, 1024, out, 2048, 1024);
}
```

```cpp
#include <hip/hip_runtime.h>
#include <stdint.h>

typedef unsigned short u16;
typedef __attribute__((ext_vector_type(8))) short bf16x8;
typedef __attribute__((ext_vector_type(4))) short s16x4;
typedef __attribute__((ext_vector_type(4))) float f32x4;
typedef __attribute__((ext_vector_type(16))) float f32x16;
typedef __attribute__((ext_vector_type(4))) unsigned u32x4;
typedef __attribute__((ext_vector_type(2))) unsigned u32x2;
typedef __attribute__((address_space(3))) s16x4* lds_s16x4_ptr;

#define DI __device__ __forceinline__

DI float bf2f(u16 v) { return __uint_as_float(((unsigned)v) << 16); }
DI u16 f2bf(float f) { __bf16 b = (__bf16)f; return __builtin_bit_cast(u16, b); }
DI float rbf(float f) { return bf2f(f2bf(f)); }

DI s16x4 tr_read(const u16* p) {
  return __builtin_amdgcn_ds_read_tr16_b64_v4i16((lds_s16x4_ptr)(p));
}

constexpr int S_ = 2048, D_ = 1024;

typedef __attribute__((address_space(3))) unsigned* lds_u32_ptr;
typedef const __attribute__((address_space(1))) unsigned* glb_u32_ptr;

DI void glds16(const void* gsrc, unsigned lds_dst) {
  unsigned keep;
  asm volatile("s_mov_b32 %0, m0\n\ts_mov_b32 m0, %2\n\ts_nop 0\n\tglobal_load_lds_dwordx4 %1, off\n\ts_mov_b32 m0, %0"
               : "=&s"(keep) : "v"(gsrc), "s"(lds_dst) : "memory");
}

template <int N>
DI void wait_vm() { asm volatile("s_waitcnt vmcnt(%0)" ::"i"(N) : "memory"); }

template <int BM, int BN, int WM, int WN, int MODE>
__global__ __launch_bounds__(WM * WN * 64) void gemm_k(const u16* __restrict__ A, int lda,
                                                       const u16* __restrict__ B, int ldb,
                                                       u16* __restrict__ C, int ldc, int K) {
  constexpr int T = WM * WN * 64;
  constexpr int BK = 64;
  constexpr int ACH = BM * 8 / T;
  constexpr int BCPR = BN / 8;
  constexpr int BCH = BK * BCPR / T;
  constexpr int NL = ACH + BCH;
  constexpr int WTM = BM / WM, WTN = BN / WN;
  constexpr int MT = WTM / 16, NT = WTN / 16;
  constexpr int ABYTES = BM * 128, BBYTES = BK * BN * 2, STAGE = ABYTES + BBYTES;
  __shared__ __attribute__((aligned(1024))) char lds[3 * STAGE];

  const int tid = threadIdx.x, lane = tid & 63, wid = tid >> 6;
  const int wm = wid / WN, wn = wid % WN;
  const int bid = blockIdx.x;
  const int xcd = bid & 7, j = bid >> 3;
  const int bm = (xcd >> 1) * 4 + (j & 3);
  const int bn = (xcd & 1) * 8 + (j >> 2);
  const int brow = bm * BM, bcol = bn * BN;

  int aoff[ACH], boff[BCH];
#pragma unroll
  for (int i = 0; i < ACH; ++i) {
    int c = tid + i * T, r = c >> 3, cp = c & 7;
    aoff[i] = (brow + r) * lda + ((cp ^ ((r >> 1) & 7)) * 8);
  }
#pragma unroll
  for (int i = 0; i < BCH; ++i) {
    int c = tid + i * T, r = c / BCPR, cp = c % BCPR;
    int sw = ((((r >> 1) & 1) | (((r >> 3) & 1) << 1)) << 1);
    boff[i] = r * ldb + bcol + ((cp ^ sw) * 8);
  }
  const unsigned ldsbase = __builtin_amdgcn_readfirstlane((unsigned)(size_t)lds + wid * 1024);
  auto stage = [&](int kt, int buf) {
    const unsigned sA = ldsbase + buf * STAGE;
    const unsigned sB = sA + ABYTES;
#pragma unroll
    for (int i = 0; i < ACH; ++i) glds16(A + aoff[i] + kt * BK, sA + i * T * 16);
#pragma unroll
    for (int i = 0; i < BCH; ++i) glds16(B + boff[i] + (size_t)kt * BK * ldb, sB + i * T * 16);
  };

  f32x4 acc[MT][NT];
#pragma unroll
  for (int m = 0; m < MT; ++m)
#pragma unroll
    for (int n = 0; n < NT; ++n) acc[m][n] = (f32x4){0.f, 0.f, 0.f, 0.f};

  const int nt = K / BK;
  stage(0, 0);
  stage(1, 1);
  const int g = lane >> 4, q = (lane & 15) >> 2, p = lane & 3;
  int aro[MT], bro[NT];
#pragma unroll
  for (int m = 0; m < MT; ++m) {
    int r = wm * WTM + m * 16 + (lane & 15);
    aro[m] = r * 128;
  }
  const int asw = (((wm * WTM + (lane & 15)) >> 1) & 7);
  const int brow0 = 8 * g + q;
  const int bsw = ((((brow0 >> 1) & 1) | (((brow0 >> 3) & 1) << 1)) << 1);
#pragma unroll
  for (int n = 0; n < NT; ++n) {
    int col = wn * WTN + n * 16 + 4 * p;
    bro[n] = brow0 * (BN * 2) + (((col >> 3) ^ bsw) * 16) + ((col >> 2) & 1) * 8;
  }
  int buf = 0;
  for (int kt = 0; kt < nt; ++kt) {
    if (kt + 1 < nt) wait_vm<NL>(); else wait_vm<0>();
    __builtin_amdgcn_s_barrier();
    int nb = buf + 2; if (nb >= 3) nb -= 3;
    if (kt + 2 < nt) stage(kt + 2, nb);
    const char* sA = lds + buf * STAGE;
    const char* sB = sA + ABYTES;
#pragma unroll
    for (int ks = 0; ks < 2; ++ks) {
      bf16x8 af[MT];
#pragma unroll
      for (int m = 0; m < MT; ++m)
        af[m] = *(const bf16x8*)(sA + aro[m] + (((ks * 4 + g) ^ asw) * 16));
      bf16x8 bfr[NT];
#pragma unroll
      for (int n = 0; n < NT; ++n) {
        const char* pb = sB + bro[n] + ks * 32 * (BN * 2);
        s16x4 lo = tr_read((const u16*)pb);
        s16x4 hi = tr_read((const u16*)(pb + 4 * (BN * 2)));
        bfr[n] = __builtin_shufflevector(lo, hi, 0, 1, 2, 3, 4, 5, 6, 7);
      }
#pragma unroll
      for (int m = 0; m < MT; ++m)
#pragma unroll
        for (int n = 0; n < NT; ++n)
          acc[m][n] = __builtin_amdgcn_mfma_f32_16x16x32_bf16(af[m], bfr[n], acc[m][n], 0, 0, 0);
    }
    buf = buf + 1; if (buf >= 3) buf = 0;
  }
  __syncthreads();
  constexpr int CS = BN + 8;
  u16* sC = (u16*)lds;
#pragma unroll
  for (int m = 0; m < MT; ++m)
#pragma unroll
    for (int n = 0; n < NT; ++n)
#pragma unroll
      for (int jj = 0; jj < 4; ++jj) {
        int r = wm * WTM + m * 16 + g * 4 + jj;
        int c = wn * WTN + n * 16 + (lane & 15);
        float t = rbf(acc[m][n][jj]);
        if (MODE == 1) {
          if (bcol + c < 2048) {
            float r1 = (t > 0.f) ? rbf(t + 1.0f) : rbf(expf(t));
            t = rbf(r1 + 1e-6f);
          }
        }
        sC[r * CS + c] = f2bf(t);
      }
  __syncthreads();
  for (int c = tid; c < BM * (BN / 8); c += T) {
    int r = c / (BN / 8), cc = c % (BN / 8);
    *(u32x4*)(C + (size_t)(brow + r) * ldc + bcol + cc * 8) = *(const u32x4*)(sC + r * CS + cc * 8);
  }
}

DI bf16x8 trfrag32(const u16* base, int stride, int ks, int lane) {
  const int h = lane >> 5, blk = (lane >> 4) & 1, q = (lane & 15) >> 2, p = lane & 3;
  const u16* pa = base + (16 * ks + 8 * h + q) * stride + 16 * blk + 4 * p;
  s16x4 lo = tr_read(pa);
  s16x4 hi = tr_read(pa + 4 * stride);
  return __builtin_shufflevector(lo, hi, 0, 1, 2, 3, 4, 5, 6, 7);
}

__global__ __launch_bounds__(256) void kvpart_k(const u16* __restrict__ qkv, float* __restrict__ part,
                                                float* __restrict__ zpart) {
  __shared__ __attribute__((aligned(16))) u16 spk[256 * 40];
  __shared__ __attribute__((aligned(16))) u16 sv[256 * 40];
  __shared__ float red[4 * 1056];
  const int h = blockIdx.x & 31, sp = blockIdx.x >> 5, t = threadIdx.x, lane = t & 63, w = t >> 6;
#pragma unroll
  for (int i = 0; i < 4; ++i) {
    int c = t + i * 256, r = c >> 2, cc = c & 3;
    const u16* rowp = qkv + (size_t)(sp * 256 + r) * 3072;
    *(u32x4*)(spk + r * 40 + cc * 8) = *(const u32x4*)(rowp + 1024 + h * 32 + cc * 8);
    *(u32x4*)(sv + r * 40 + cc * 8) = *(const u32x4*)(rowp + 2048 + h * 32 + cc * 8);
  }
  __syncthreads();
  f32x16 akv, az;
#pragma unroll
  for (int i = 0; i < 16; ++i) { akv[i] = 0.f; az[i] = 0.f; }
  bf16x8 ones;
#pragma unroll
  for (int i = 0; i < 8; ++i) ones[i] = (short)0x3f80;
#pragma unroll
  for (int ks = 0; ks < 4; ++ks) {
    bf16x8 a = trfrag32(spk + w * 64 * 40, 40, ks, lane);
    bf16x8 b = trfrag32(sv + w * 64 * 40, 40, ks, lane);
    akv = __builtin_amdgcn_mfma_f32_32x32x16_bf16(a, b, akv, 0, 0, 0);
    az = __builtin_amdgcn_mfma_f32_32x32x16_bf16(a, ones, az, 0, 0, 0);
  }
  const int n = lane & 31, hh = lane >> 5;
#pragma unroll
  for (int i = 0; i < 16; ++i) {
    int row = (i & 3) + 8 * (i >> 2) + 4 * hh;
    red[w * 1056 + row * 32 + n] = akv[i];
    if (n == 0) red[w * 1056 + 1024 + row] = az[i];
  }
  __syncthreads();
  for (int i = t; i < 1056; i += 256) {
    float s = red[i] + red[1056 + i] + red[2112 + i] + red[3168 + i];
    if (i < 1024) part[(size_t)(sp * 32 + h) * 1024 + i] = s;
    else zpart[(sp * 32 + h) * 32 + (i - 1024)] = s;
  }
}

__global__ __launch_bounds__(256) void o_k(const u16* __restrict__ qkv, const float* __restrict__ part,
                                           const float* __restrict__ zpart, u16* __restrict__ o) {
  __shared__ __attribute__((aligned(16))) u16 skvT[2 * 32 * 40];
  __shared__ __attribute__((aligned(16))) u16 sz[2 * 32];
  const int rb = blockIdx.x >> 4, hg = blockIdx.x & 15, t = threadIdx.x, lane = t & 63, w = t >> 6;
  for (int i = t; i < 2 * 1024; i += 256) {
    int hh = i >> 10, de = i & 1023;
    float s = 0;
#pragma unroll
    for (int sp = 0; sp < 8; ++sp) s += part[(size_t)(sp * 32 + hg * 2 + hh) * 1024 + de];
    skvT[hh * 1280 + (de & 31) * 40 + (de >> 5)] = f2bf(s);
  }
  if (t < 64) {
    int hh = t >> 5, d = t & 31;
    float s = 0;
#pragma unroll
    for (int sp = 0; sp < 8; ++sp) s += zpart[(sp * 32 + hg * 2 + hh) * 32 + d];
    sz[t] = f2bf(s);
  }
  __syncthreads();
  const int n = lane & 31, hf = lane >> 5;
  const int row0 = rb * 128 + w * 32;
#pragma unroll
  for (int hh = 0; hh < 2; ++hh) {
    f32x16 an, ad;
#pragma unroll
    for (int i = 0; i < 16; ++i) { an[i] = 0.f; ad[i] = 0.f; }
#pragma unroll
    for (int ks = 0; ks < 2; ++ks) {
      bf16x8 a = *(const bf16x8*)(qkv + (size_t)(row0 + n) * 3072 + (hg * 2 + hh) * 32 + ks * 16 + 8 * hf);
      bf16x8 b = *(const bf16x8*)(skvT + hh * 1280 + n * 40 + ks * 16 + 8 * hf);
      bf16x8 bz = *(const bf16x8*)(sz + hh * 32 + ks * 16 + 8 * hf);
      an = __builtin_amdgcn_mfma_f32_32x32x16_bf16(a, b, an, 0, 0, 0);
      ad = __builtin_amdgcn_mfma_f32_32x32x16_bf16(a, bz, ad, 0, 0, 0);
    }
#pragma unroll
    for (int i = 0; i < 16; ++i) {
      int row = (i & 3) + 8 * (i >> 2) + 4 * hf;
      float den = rbf(rbf(ad[i]) + 1e-6f);
      o[(size_t)(row0 + row) * 1024 + (hg * 2 + hh) * 32 + n] = f2bf(rbf(an[i]) / den);
    }
  }
}

__global__ __launch_bounds__(256) void pre_k(const u16* __restrict__ k, const u16* __restrict__ v,
                                             const u16* __restrict__ shk, u16* __restrict__ uT) {
  __shared__ __attribute__((aligned(16))) u16 tile[64 * 72];
  const int sb = blockIdx.x >> 4, db = blockIdx.x & 15, t = threadIdx.x;
  const int cc = t & 7, d0 = db * 64 + cc * 8;
  float sh[8][4];
#pragma unroll
  for (int i = 0; i < 8; ++i)
#pragma unroll
    for (int j = 0; j < 4; ++j) sh[i][j] = bf2f(shk[(d0 + i) * 4 + j]);
#pragma unroll
  for (int rr = 0; rr < 2; ++rr) {
    int r = (t >> 3) + rr * 32, s = sb * 64 + r;
    float acc[8];
#pragma unroll
    for (int i = 0; i < 8; ++i) acc[i] = 0.f;
#pragma unroll
    for (int tt = 0; tt < 4; ++tt) {
      int ss = s + tt - 3;
      if (ss >= 0) {
        u32x4 kk = *(const u32x4*)(k + (size_t)ss * D_ + d0);
#pragma unroll
        for (int i = 0; i < 8; ++i) {
          float kvv = (i & 1) ? __uint_as_float(kk[i >> 1] & 0xffff0000u) : __uint_as_float(kk[i >> 1] << 16);
          acc[i] = rbf(acc[i] + rbf(kvv * sh[i][3 - tt]));
        }
      }
    }
    u32x4 vv = *(const u32x4*)(v + (size_t)s * D_ + d0);
#pragma unroll
    for (int i = 0; i < 8; ++i) {
      float vf = (i & 1) ? __uint_as_float(vv[i >> 1] & 0xffff0000u) : __uint_as_float(vv[i >> 1] << 16);
      tile[(cc * 8 + i) * 72 + r] = f2bf(acc[i] * vf);
    }
  }
  __syncthreads();
  for (int c = t; c < 64 * 8; c += 256) {
    int d = c >> 3, c8 = c & 7;
    *(u32x4*)(uT + (size_t)(db * 64 + d) * S_ + sb * 64 + c8 * 8) = *(const u32x4*)(tile + d * 72 + c8 * 8);
  }
}

constexpr int WS = 328;
constexpr int HS = 2112;
constexpr int CW = 8 * WS + 64 * 72 + HS;
__global__ __launch_bounds__(256) void conv_k(const u16* __restrict__ uT, const u16* __restrict__ hk,
                                              u16* __restrict__ yT) {
  __shared__ __attribute__((aligned(16))) u16 lds[4 * CW];
  const int t = threadIdx.x, lane = t & 63, w = t >> 6;
  const int d = blockIdx.x * 4 + w;
  u16* G = lds + w * CW;
  u16* U = G + 8 * WS;
  u16* HR = U + 64 * 72;
  const u16* h = hk + (size_t)d * S_;
#pragma unroll
  for (int i = 0; i < 4; ++i) {
    int c = lane + i * 64;
    u32x4 v = *(const u32x4*)(h + c * 8);
#pragma unroll
    for (int j = 0; j < 4; ++j) {
      HR[2047 - (c * 8 + 2 * j)] = (u16)(v[j] & 0xffff);
      HR[2047 - (c * 8 + 2 * j + 1)] = (u16)(v[j] >> 16);
    }
  }
  HR[2048 + lane] = 0;
  for (int idx = lane; idx < 64 * 9; idx += 64) {
    int b = idx / 9, c8 = idx - b * 9;
    u32x4 val = (u32x4){0u, 0u, 0u, 0u};
    if (b >= 32 && c8 < 8) val = *(const u32x4*)(uT + (size_t)d * S_ + (b - 32) * 64 + c8 * 8);
    *(u32x4*)(U + b * 72 + c8 * 8) = val;
  }
  __syncthreads();
  f32x16 acc0, acc1;
#pragma unroll
  for (int i = 0; i < 16; ++i) { acc0[i] = 0.f; acc1[i] = 0.f; }
  const int n = lane & 31, hh = lane >> 5;
  for (int m0 = 0; m0 < 32; m0 += 4) {
    const int xb = 1792 - 64 * m0;
#pragma unroll
    for (int i = 0; i < 5; ++i) {
      int y = lane + i * 64;
      u16 v = HR[xb + y];
#pragma unroll
      for (int s = 0; s < 8; ++s)
        if (y - s >= 0) G[s * WS + y - s] = v;
    }
    __syncthreads();
#pragma unroll
    for (int mm = 0; mm < 4; ++mm) {
      const int m = m0 + mm;
#pragma unroll
      for (int kk = 0; kk < 4; ++kk) {
        bf16x8 bfr = *(const bf16x8*)(U + (n - m + 32) * 72 + kk * 16 + 8 * hh);
        int x0 = 2047 - 64 * m - n + kk * 16 + 8 * hh;
        int s0 = x0 & 7;
        const u16* gp = G + s0 * WS + (x0 - s0 - xb);
        bf16x8 a0 = *(const bf16x8*)(gp);
        bf16x8 a1 = *(const bf16x8*)(gp - 32);
        acc0 = __builtin_amdgcn_mfma_f32_32x32x16_bf16(a0, bfr, acc0, 0, 0, 0);
        acc1 = __builtin_amdgcn_mfma_f32_32x32x16_bf16(a1, bfr, acc1, 0, 0, 0);
      }
    }
    __syncthreads();
  }
#pragma unroll
  for (int i = 0; i < 16; ++i) {
    int row = (i & 3) + 8 * (i >> 2) + 4 * hh;
    HR[n * 64 + row] = f2bf(acc0[i]);
    HR[n * 64 + 32 + row] = f2bf(acc1[i]);
  }
  __syncthreads();
#pragma unroll
  for (int i = 0; i < 4; ++i) {
    int c = lane + i * 64;
    *(u32x4*)(yT + (size_t)d * S_ + c * 8) = *(const u32x4*)(HR + c * 8);
  }
}

__global__ __launch_bounds__(256) void post_k(const u16* __restrict__ uT, const u16* __restrict__ yT,
                                              const u16* __restrict__ q, const u16* __restrict__ Dv,
                                              u16* __restrict__ out) {
  __shared__ __attribute__((aligned(16))) u16 tu[64 * 72];
  __shared__ __attribute__((aligned(16))) u16 ty[64 * 72];
  const int sb = blockIdx.x >> 4, db = blockIdx.x & 15, t = threadIdx.x;
  for (int c = t; c < 64 * 8; c += 256) {
    int d = c >> 3, c8 = c & 7;
    *(u32x4*)(tu + d * 72 + c8 * 8) = *(const u32x4*)(uT + (size_t)(db * 64 + d) * S_ + sb * 64 + c8 * 8);
    *(u32x4*)(ty + d * 72 + c8 * 8) = *(const u32x4*)(yT + (size_t)(db * 64 + d) * S_ + sb * 64 + c8 * 8);
  }
  __syncthreads();
  const int cc = t & 7, d0 = db * 64 + cc * 8;
#pragma unroll
  for (int rr = 0; rr < 2; ++rr) {
    int r = (t >> 3) + rr * 32, s = sb * 64 + r;
    u32x4 qq = *(const u32x4*)(q + (size_t)s * D_ + d0);
    u16 ov[8];
#pragma unroll
    for (int i = 0; i < 8; ++i) {
      float qf = (i & 1) ? __uint_as_float(qq[i >> 1] & 0xffff0000u) : __uint_as_float(qq[i >> 1] << 16);
      float kvv = bf2f(tu[(cc * 8 + i) * 72 + r]);
      float y = bf2f(ty[(cc * 8 + i) * 72 + r]);
      float y2 = rbf(y + rbf(kvv * bf2f(Dv[d0 + i])));
      ov[i] = f2bf(qf * y2);
    }
    u32x4 w;
#pragma unroll
    for (int k = 0; k < 4; ++k) w[k] = (unsigned)ov[2 * k] | ((unsigned)ov[2 * k + 1] << 16);
    *(u32x4*)(out + (size_t)s * 2048 + 1024 + d0) = w;
  }
}

extern "C" void kernel_launch(void* const* d_in, const int* in_sizes, int n_in, void* d_out, int out_size,
                              void* d_ws, size_t ws_size, hipStream_t stream) {
  const u16* x = (const u16*)d_in[0];
  const u16* wqkv = (const u16*)d_in[1];
  const u16* wo = (const u16*)d_in[2];
  const u16* bq = (const u16*)d_in[3];
  const u16* bk = (const u16*)d_in[4];
  const u16* bv = (const u16*)d_in[5];
  const u16* shk = (const u16*)d_in[6];
  const u16* ssk = (const u16*)d_in[7];
  const u16* ssD = (const u16*)d_in[8];
  u16* out = (u16*)d_out;
  char* ws = (char*)d_ws;
  u16* qkv = (u16*)(ws);
  float* part = (float*)(ws + (12u << 20));
  float* zpart = (float*)(ws + (13u << 20));
  u16* o = (u16*)(ws + (14u << 20));
  u16* uT = (u16*)(ws + (18u << 20));
  u16* yT = (u16*)(ws + (22u << 20));

  gemm_k<128, 192, 2, 4, 1><<<256, 512, 0, stream>>>(x, 1024, wqkv, 3072, qkv, 3072, 1024);
  pre_k<<<512, 256, 0, stream>>>(bk, bv, shk, uT);
  kvpart_k<<<256, 256, 0, stream>>>(qkv, part, zpart);
  conv_k<<<256, 256, 0, stream>>>(uT, ssk, yT);
  o_k<<<256, 256, 0, stream>>>(qkv, part, zpart, o);
  post_k<<<512, 256, 0, stream>>>(uT, yT, bq, ssD, out);
  gemm_k<128, 64, 2, 2, 0><<<256, 256, 0, stream>>>(o, 1024, wo, 1024, out, 2048, 1024);
}
```

```cpp
#include <hip/hip_runtime.h>
#include <stdint.h>

typedef unsigned short u16;
typedef __attribute__((ext_vector_type(8))) short bf16x8;
typedef __attribute__((ext_vector_type(4))) short s16x4;
typedef __attribute__((ext_vector_type(4))) float f32x4;
typedef __attribute__((ext_vector_type(16))) float f32x16;
typedef __attribute__((ext_vector_type(4))) unsigned u32x4;
typedef __attribute__((ext_vector_type(2))) unsigned u32x2;
typedef __attribute__((address_space(3))) s16x4* lds_s16x4_ptr;

#define DI __device__ __forceinline__

DI float bf2f(u16 v) { return __uint_as_float(((unsigned)v) << 16); }
DI u16 f2bf(float f) { __bf16 b = (__bf16)f; return __builtin_bit_cast(u16, b); }
DI float rbf(float f) { return bf2f(f2bf(f)); }

DI s16x4 tr_read(const u16* p) {
  return __builtin_amdgcn_ds_read_tr16_b64_v4i16((lds_s16x4_ptr)(p));
}

constexpr int S_ = 2048, D_ = 1024;

typedef __attribute__((address_space(3))) unsigned* lds_u32_ptr;
typedef const __attribute__((address_space(1))) unsigned* glb_u32_ptr;

DI void glds16(const void* gsrc, unsigned lds_dst) {
  unsigned keep;
  asm volatile("s_mov_b32 %0, m0\n\ts_mov_b32 m0, %2\n\ts_nop 0\n\tglobal_load_lds_dwordx4 %1, off\n\ts_mov_b32 m0, %0"
               : "=&s"(keep) : "v"(gsrc), "s"(lds_dst) : "memory");
}

template <int N>
DI void wait_vm() { asm volatile("s_waitcnt vmcnt(%0)" ::"i"(N) : "memory"); }

template <int BM, int BN, int WM, int WN, int MODE, int NST>
DI void gemm_body(char* lds, const int bid, const u16* __restrict__ A, int lda, const u16* __restrict__ B, int ldb,
                  u16* __restrict__ C, int ldc, int K) {
  constexpr int T = WM * WN * 64;
  constexpr int BK = 64;
  constexpr int ACH = BM * 8 / T;
  constexpr int BCPR = BN / 8;
  constexpr int BCH = BK * BCPR / T;
  constexpr int NL = ACH + BCH;
  constexpr int WTM = BM / WM, WTN = BN / WN;
  constexpr int MT = WTM / 16, NT = WTN / 16;
  constexpr int ABYTES = BM * 128, BBYTES = BK * BN * 2, STAGE = ABYTES + BBYTES;

  const int tid = threadIdx.x, lane = tid & 63, wid = tid >> 6;
  const int wm = wid / WN, wn = wid % WN;
  const int xcd = bid & 7, j = bid >> 3;
  const int bm = (xcd >> 1) * 4 + (j & 3);
  const int bn = (xcd & 1) * 8 + (j >> 2);
  const int brow = bm * BM, bcol = bn * BN;

  int aoff[ACH], boff[BCH];
#pragma unroll
  for (int i = 0; i < ACH; ++i) {
    int c = tid + i * T, r = c >> 3, cp = c & 7;
    aoff[i] = (brow + r) * lda + ((cp ^ ((r >> 1) & 7)) * 8);
  }
#pragma unroll
  for (int i = 0; i < BCH; ++i) {
    int c = tid + i * T, r = c / BCPR, cp = c % BCPR;
    int sw = ((((r >> 1) & 1) | (((r >> 3) & 1) << 1)) << 1);
    boff[i] = r * ldb + bcol + ((cp ^ sw) * 8);
  }
  const unsigned ldsbase = __builtin_amdgcn_readfirstlane((unsigned)(size_t)lds + wid * 1024);
  auto stage = [&](int kt, int buf) {
    const unsigned sA = ldsbase + buf * STAGE;
    const unsigned sB = sA + ABYTES;
#pragma unroll
    for (int i = 0; i < ACH; ++i) glds16(A + aoff[i] + kt * BK, sA + i * T * 16);
#pragma unroll
    for (int i = 0; i < BCH; ++i) glds16(B + boff[i] + (size_t)kt * BK * ldb, sB + i * T * 16);
  };

  f32x4 acc[MT][NT];
#pragma unroll
  for (int m = 0; m < MT; ++m)
#pragma unroll
    for (int n = 0; n < NT; ++n) acc[m][n] = (f32x4){0.f, 0.f, 0.f, 0.f};

  const int nt = K / BK;
#pragma unroll
  for (int i = 0; i < NST - 1; ++i) stage(i, i);
  const int g = lane >> 4, q = (lane & 15) >> 2, p = lane & 3;
  int aro[MT], bro[NT];
#pragma unroll
  for (int m = 0; m < MT; ++m) {
    int r = wm * WTM + m * 16 + (lane & 15);
    aro[m] = r * 128;
  }
  const int asw = (((wm * WTM + (lane & 15)) >> 1) & 7);
  const int brow0 = 8 * g + q;
  const int bsw = ((((brow0 >> 1) & 1) | (((brow0 >> 3) & 1) << 1)) << 1);
#pragma unroll
  for (int n = 0; n < NT; ++n) {
    int col = wn * WTN + n * 16 + 4 * p;
    bro[n] = brow0 * (BN * 2) + (((col >> 3) ^ bsw) * 16) + ((col >> 2) & 1) * 8;
  }
  int buf = 0;
  for (int kt = 0; kt < nt; ++kt) {
    if (NST == 3 && kt + 1 < nt) wait_vm<NL>(); else wait_vm<0>();
    __builtin_amdgcn_s_barrier();
    int nb = buf + NST - 1; if (nb >= NST) nb -= NST;
    if (kt + NST - 1 < nt) stage(kt + NST - 1, nb);
    const char* sA = lds + buf * STAGE;
    const char* sB = sA + ABYTES;
#pragma unroll
    for (int ks = 0; ks < 2; ++ks) {
      bf16x8 af[MT];
#pragma unroll
      for (int m = 0; m < MT; ++m)
        af[m] = *(const bf16x8*)(sA + aro[m] + (((ks * 4 + g) ^ asw) * 16));
      bf16x8 bfr[NT];
#pragma unroll
      for (int n = 0; n < NT; ++n) {
        const char* pb = sB + bro[n] + ks * 32 * (BN * 2);
        s16x4 lo = tr_read((const u16*)pb);
        s16x4 hi = tr_read((const u16*)(pb + 4 * (BN * 2)));
        bfr[n] = __builtin_shufflevector(lo, hi, 0, 1, 2, 3, 4, 5, 6, 7);
      }
#pragma unroll
      for (int m = 0; m < MT; ++m)
#pragma unroll
        for (int n = 0; n < NT; ++n)
          acc[m][n] = __builtin_amdgcn_mfma_f32_16x16x32_bf16(af[m], bfr[n], acc[m][n], 0, 0, 0);
    }
    buf = buf + 1; if (buf >= NST) buf = 0;
  }
  __syncthreads();
  constexpr int CS = BN + 8;
  u16* sC = (u16*)lds;
#pragma unroll
  for (int m = 0; m < MT; ++m)
#pragma unroll
    for (int n = 0; n < NT; ++n)
#pragma unroll
      for (int jj = 0; jj < 4; ++jj) {
        int r = wm * WTM + m * 16 + g * 4 + jj;
        int c = wn * WTN + n * 16 + (lane & 15);
        float t = rbf(acc[m][n][jj]);
        if (MODE == 1) {
          float r1 = (t > 0.f) ? rbf(t + 1.0f) : rbf(__expf(t));
          r1 = rbf(r1 + 1e-6f);
          t = (bcol + c < 2048) ? r1 : t;
        }
        sC[r * CS + c] = f2bf(t);
      }
  __syncthreads();
  for (int c = tid; c < BM * (BN / 8); c += T) {
    int r = c / (BN / 8), cc = c % (BN / 8);
    *(u32x4*)(C + (size_t)(brow + r) * ldc + bcol + cc * 8) = *(const u32x4*)(sC + r * CS + cc * 8);
  }
}

DI bf16x8 trfrag32(const u16* base, int stride, int ks, int lane) {
  const int h = lane >> 5, blk = (lane >> 4) & 1, q = (lane & 15) >> 2, p = lane & 3;
  const u16* pa = base + (16 * ks + 8 * h + q) * stride + 16 * blk + 4 * p;
  s16x4 lo = tr_read(pa);
  s16x4 hi = tr_read(pa + 4 * stride);
  return __builtin_shufflevector(lo, hi, 0, 1, 2, 3, 4, 5, 6, 7);
}

__global__ __launch_bounds__(256) void kvpart_k(const u16* __restrict__ qkv, float* __restrict__ part,
                                                float* __restrict__ zpart) {
  __shared__ __attribute__((aligned(16))) u16 spk[256 * 40];
  __shared__ __attribute__((aligned(16))) u16 sv[256 * 40];
  __shared__ float red[4 * 1056];
  const int h = blockIdx.x & 31, sp = blockIdx.x >> 5, t = threadIdx.x, lane = t & 63, w = t >> 6;
#pragma unroll
  for (int i = 0; i < 4; ++i) {
    int c = t + i * 256, r = c >> 2, cc = c & 3;
    const u16* rowp = qkv + (size_t)(sp * 256 + r) * 3072;
    *(u32x4*)(spk + r * 40 + cc * 8) = *(const u32x4*)(rowp + 1024 + h * 32 + cc * 8);
    *(u32x4*)(sv + r * 40 + cc * 8) = *(const u32x4*)(rowp + 2048 + h * 32 + cc * 8);
  }
  __syncthreads();
  f32x16 akv, az;
#pragma unroll
  for (int i = 0; i < 16; ++i) { akv[i] = 0.f; az[i] = 0.f; }
  bf16x8 ones;
#pragma unroll
  for (int i = 0; i < 8; ++i) ones[i] = (short)0x3f80;
#pragma unroll
  for (int ks = 0; ks < 4; ++ks) {
    bf16x8 a = trfrag32(spk + w * 64 * 40, 40, ks, lane);
    bf16x8 b = trfrag32(sv + w * 64 * 40, 40, ks, lane);
    akv = __builtin_amdgcn_mfma_f32_32x32x16_bf16(a, b, akv, 0, 0, 0);
    az = __builtin_amdgcn_mfma_f32_32x32x16_bf16(a, ones, az, 0, 0, 0);
  }
  const int n = lane & 31, hh = lane >> 5;
#pragma unroll
  for (int i = 0; i < 16; ++i) {
    int row = (i & 3) + 8 * (i >> 2) + 4 * hh;
    red[w * 1056 + row * 32 + n] = akv[i];
    if (n == 0) red[w * 1056 + 1024 + row] = az[i];
  }
  __syncthreads();
  for (int i = t; i < 1056; i += 256) {
    float s = red[i] + red[1056 + i] + red[2112 + i] + red[3168 + i];
    if (i < 1024) part[(size_t)(sp * 32 + h) * 1024 + i] = s;
    else zpart[(sp * 32 + h) * 32 + (i - 1024)] = s;
  }
}

__global__ __launch_bounds__(256) void o_k(const u16* __restrict__ qkv, const float* __restrict__ part,
                                           const float* __restrict__ zpart, u16* __restrict__ o) {
  __shared__ __attribute__((aligned(16))) u16 skvT[2 * 32 * 40];
  __shared__ __attribute__((aligned(16))) u16 sz[2 * 32];
  const int rb = blockIdx.x >> 4, hg = blockIdx.x & 15, t = threadIdx.x, lane = t & 63, w = t >> 6;
  for (int i = t; i < 2 * 1024; i += 256) {
    int hh = i >> 10, de = i & 1023;
    float s = 0;
#pragma unroll
    for (int sp = 0; sp < 8; ++sp) s += part[(size_t)(sp * 32 + hg * 2 + hh) * 1024 + de];
    skvT[hh * 1280 + (de & 31) * 40 + (de >> 5)] = f2bf(s);
  }
  if (t < 64) {
    int hh = t >> 5, d = t & 31;
    float s = 0;
#pragma unroll
    for (int sp = 0; sp < 8; ++sp) s += zpart[(sp * 32 + hg * 2 + hh) * 32 + d];
    sz[t] = f2bf(s);
  }
  __syncthreads();
  const int n = lane & 31, hf = lane >> 5;
  const int row0 = rb * 128 + w * 32;
#pragma unroll
  for (int hh = 0; hh < 2; ++hh) {
    f32x16 an, ad;
#pragma unroll
    for (int i = 0; i < 16; ++i) { an[i] = 0.f; ad[i] = 0.f; }
#pragma unroll
    for (int ks = 0; ks < 2; ++ks) {
      bf16x8 a = *(const bf16x8*)(qkv + (size_t)(row0 + n) * 3072 + (hg * 2 + hh) * 32 + ks * 16 + 8 * hf);
      bf16x8 b = *(const bf16x8*)(skvT + hh * 1280 + n * 40 + ks * 16 + 8 * hf);
      bf16x8 bz = *(const bf16x8*)(sz + hh * 32 + ks * 16 + 8 * hf);
      an = __builtin_amdgcn_mfma_f32_32x32x16_bf16(a, b, an, 0, 0, 0);
      ad = __builtin_amdgcn_mfma_f32_32x32x16_bf16(a, bz, ad, 0, 0, 0);
    }
#pragma unroll
    for (int i = 0; i < 16; ++i) {
      int row = (i & 3) + 8 * (i >> 2) + 4 * hf;
      float den = rbf(rbf(ad[i]) + 1e-6f);
      o[(size_t)(row0 + row) * 1024 + (hg * 2 + hh) * 32 + n] = f2bf(rbf(an[i]) / den);
    }
  }
}

__global__ __launch_bounds__(256) void pre_k(const u16* __restrict__ k, const u16* __restrict__ v,
                                             const u16* __restrict__ shk, u16* __restrict__ uT) {
  __shared__ __attribute__((aligned(16))) u16 tile[64 * 72];
  const int sb = blockIdx.x >> 4, db = blockIdx.x & 15, t = threadIdx.x;
  const int cc = t & 7, d0 = db * 64 + cc * 8;
  float sh[8][4];
#pragma unroll
  for (int i = 0; i < 8; ++i)
#pragma unroll
    for (int j = 0; j < 4; ++j) sh[i][j] = bf2f(shk[(d0 + i) * 4 + j]);
#pragma unroll
  for (int rr = 0; rr < 2; ++rr) {
    int r = (t >> 3) + rr * 32, s = sb * 64 + r;
    float acc[8];
#pragma unroll
    for (int i = 0; i < 8; ++i) acc[i] = 0.f;
#pragma unroll
    for (int tt = 0; tt < 4; ++tt) {
      int ss = s + tt - 3;
      if (ss >= 0) {
        u32x4 kk = *(const u32x4*)(k + (size_t)ss * D_ + d0);
#pragma unroll
        for (int i = 0; i < 8; ++i) {
          float kvv = (i & 1) ? __uint_as_float(kk[i >> 1] & 0xffff0000u) : __uint_as_float(kk[i >> 1] << 16);
          acc[i] = rbf(acc[i] + rbf(kvv * sh[i][3 - tt]));
        }
      }
    }
    u32x4 vv = *(const u32x4*)(v + (size_t)s * D_ + d0);
#pragma unroll
    for (int i = 0; i < 8; ++i) {
      float vf = (i & 1) ? __uint_as_float(vv[i >> 1] & 0xffff0000u) : __uint_as_float(vv[i >> 1] << 16);
      tile[(cc * 8 + i) * 72 + r] = f2bf(acc[i] * vf);
    }
  }
  __syncthreads();
  for (int c = t; c < 64 * 8; c += 256) {
    int d = c >> 3, c8 = c & 7;
    *(u32x4*)(uT + (size_t)(db * 64 + d) * S_ + sb * 64 + c8 * 8) = *(const u32x4*)(tile + d * 72 + c8 * 8);
  }
}

constexpr int WS = 328;
constexpr int HS = 2112;
constexpr int CW = 8 * WS + 64 * 72 + HS;
DI void wsync() { asm volatile("s_waitcnt lgkmcnt(0)" ::: "memory"); __builtin_amdgcn_wave_barrier(); }
DI void conv_body(u16* lds, const int bid, const u16* __restrict__ uT, const u16* __restrict__ hk,
                  u16* __restrict__ yT) {
  const int t = threadIdx.x, lane = t & 63, w = t >> 6;
  if (w >= 4) return;
  const int d = bid * 4 + w;
  u16* G = lds + w * CW;
  u16* U = G + 8 * WS;
  u16* HR = U + 64 * 72;
  const u16* h = hk + (size_t)d * S_;
#pragma unroll
  for (int i = 0; i < 4; ++i) {
    int c = lane + i * 64;
    u32x4 v = *(const u32x4*)(h + c * 8);
#pragma unroll
    for (int j = 0; j < 4; ++j) {
      HR[2047 - (c * 8 + 2 * j)] = (u16)(v[j] & 0xffff);
      HR[2047 - (c * 8 + 2 * j + 1)] = (u16)(v[j] >> 16);
    }
  }
  HR[2048 + lane] = 0;
  for (int idx = lane; idx < 64 * 9; idx += 64) {
    int b = idx / 9, c8 = idx - b * 9;
    u32x4 val = (u32x4){0u, 0u, 0u, 0u};
    if (b >= 32 && c8 < 8) val = *(const u32x4*)(uT + (size_t)d * S_ + (b - 32) * 64 + c8 * 8);
    *(u32x4*)(U + b * 72 + c8 * 8) = val;
  }
  wsync();
  f32x16 acc0, acc1;
#pragma unroll
  for (int i = 0; i < 16; ++i) { acc0[i] = 0.f; acc1[i] = 0.f; }
  const int n = lane & 31, hh = lane >> 5;
  for (int m0 = 0; m0 < 32; m0 += 4) {
    const int xb = 1792 - 64 * m0;
#pragma unroll
    for (int i = 0; i < 5; ++i) {
      int y = lane + i * 64;
      u16 v = HR[xb + y];
#pragma unroll
      for (int s = 0; s < 8; ++s)
        if (y - s >= 0) G[s * WS + y - s] = v;
    }
    wsync();
#pragma unroll
    for (int mm = 0; mm < 4; ++mm) {
      const int m = m0 + mm;
#pragma unroll
      for (int kk = 0; kk < 4; ++kk) {
        bf16x8 bfr = *(const bf16x8*)(U + (n - m + 32) * 72 + kk * 16 + 8 * hh);
        int x0 = 2047 - 64 * m - n + kk * 16 + 8 * hh;
        int s0 = x0 & 7;
        const u16* gp = G + s0 * WS + (x0 - s0 - xb);
        bf16x8 a0 = *(const bf16x8*)(gp);
        bf16x8 a1 = *(const bf16x8*)(gp - 32);
        acc0 = __builtin_amdgcn_mfma_f32_32x32x16_bf16(a0, bfr, acc0, 0, 0, 0);
        acc1 = __builtin_amdgcn_mfma_f32_32x32x16_bf16(a1, bfr, acc1, 0, 0, 0);
      }
    }
    wsync();
  }
#pragma unroll
  for (int i = 0; i < 16; ++i) {
    int row = (i & 3) + 8 * (i >> 2) + 4 * hh;
    HR[n * 64 + row] = f2bf(acc0[i]);
    HR[n * 64 + 32 + row] = f2bf(acc1[i]);
  }
  wsync();
#pragma unroll
  for (int i = 0; i < 4; ++i) {
    int c = lane + i * 64;
    *(u32x4*)(yT + (size_t)d * S_ + c * 8) = *(const u32x4*)(HR + c * 8);
  }
}

DI void post_body(u16* lds, const int bid, const u16* __restrict__ uT, const u16* __restrict__ yT,
                  const u16* __restrict__ q, const u16* __restrict__ Dv, u16* __restrict__ out) {
  u16* tu = lds;
  u16* ty = lds + 64 * 72;
  const int sb = bid >> 4, db = bid & 15, t = threadIdx.x;
  for (int c = t; c < 64 * 8; c += 256) {
    int d = c >> 3, c8 = c & 7;
    *(u32x4*)(tu + d * 72 + c8 * 8) = *(const u32x4*)(uT + (size_t)(db * 64 + d) * S_ + sb * 64 + c8 * 8);
    *(u32x4*)(ty + d * 72 + c8 * 8) = *(const u32x4*)(yT + (size_t)(db * 64 + d) * S_ + sb * 64 + c8 * 8);
  }
  __syncthreads();
  const int cc = t & 7, d0 = db * 64 + cc * 8;
#pragma unroll
  for (int rr = 0; rr < 2; ++rr) {
    int r = (t >> 3) + rr * 32, s = sb * 64 + r;
    u32x4 qq = *(const u32x4*)(q + (size_t)s * D_ + d0);
    u16 ov[8];
#pragma unroll
    for (int i = 0; i < 8; ++i) {
      float qf = (i & 1) ? __uint_as_float(qq[i >> 1] & 0xffff0000u) : __uint_as_float(qq[i >> 1] << 16);
      float kvv = bf2f(tu[(cc * 8 + i) * 72 + r]);
      float y = bf2f(ty[(cc * 8 + i) * 72 + r]);
      float y2 = rbf(y + rbf(kvv * bf2f(Dv[d0 + i])));
      ov[i] = f2bf(qf * y2);
    }
    u32x4 w;
#pragma unroll
    for (int k = 0; k < 4; ++k) w[k] = (unsigned)ov[2 * k] | ((unsigned)ov[2 * k + 1] << 16);
    *(u32x4*)(out + (size_t)s * 2048 + 1024 + d0) = w;
  }
}

__global__ __launch_bounds__(512) void k_gemm1_conv(const u16* __restrict__ x, const u16* __restrict__ wqkv,
                                                    u16* __restrict__ qkv, const u16* __restrict__ uT,
                                                    const u16* __restrict__ ssk, u16* __restrict__ yT) {
  __shared__ __attribute__((aligned(1024))) char lds[2 * (128 * 128 + 64 * 192 * 2)];
  if (blockIdx.x < 256) gemm_body<128, 192, 2, 4, 1, 2>(lds, blockIdx.x, x, 1024, wqkv, 3072, qkv, 3072, 1024);
  else conv_body((u16*)lds, blockIdx.x - 256, uT, ssk, yT);
}
__global__ __launch_bounds__(256) void k_gemm2_post(const u16* __restrict__ o, const u16* __restrict__ wo,
                                                    u16* __restrict__ out, const u16* __restrict__ uT,
                                                    const u16* __restrict__ yT, const u16* __restrict__ q,
                                                    const u16* __restrict__ Dv) {
  __shared__ __attribute__((aligned(1024))) char lds[3 * (128 * 128 + 64 * 64 * 2)];
  if (blockIdx.x < 256) gemm_body<128, 64, 2, 2, 0, 3>(lds, blockIdx.x, o, 1024, wo, 1024, out, 2048, 1024);
  else post_body((u16*)lds, blockIdx.x - 256, uT, yT, q, Dv, out);
}

extern "C" void kernel_launch(void* const* d_in, const int* in_sizes, int n_in, void* d_out, int out_size,
                              void* d_ws, size_t ws_size, hipStream_t stream) {
  const u16* x = (const u16*)d_in[0];
  const u16* wqkv = (const u16*)d_in[1];
  const u16* wo = (const u16*)d_in[2];
  const u16* bq = (const u16*)d_in[3];
  const u16* bk = (const u16*)d_in[4];
  const u16* bv = (const u16*)d_in[5];
  const u16* shk = (const u16*)d_in[6];
  const u16* ssk = (const u16*)d_in[7];
  const u16* ssD = (const u16*)d_in[8];
  u16* out = (u16*)d_out;
  char* ws = (char*)d_ws;
  u16* qkv = (u16*)(ws);
  float* part = (float*)(ws + (12u << 20));
  float* zpart = (float*)(ws + (13u << 20));
  u16* o = (u16*)(ws + (14u << 20));
  u16* uT = (u16*)(ws + (18u << 20));
  u16* yT = (u16*)(ws + (22u << 20));

  pre_k<<<512, 256, 0, stream>>>(bk, bv, shk, uT);
  k_gemm1_conv<<<512, 512, 0, stream>>>(x, wqkv, qkv, uT, ssk, yT);
  kvpart_k<<<256, 256, 0, stream>>>(qkv, part, zpart);
  o_k<<<256, 256, 0, stream>>>(qkv, part, zpart, o);
  k_gemm2_post<<<768, 256, 0, stream>>>(o, wo, out, uT, yT, bq, ssD);
}
```

```cpp
#include <hip/hip_runtime.h>
#include <stdint.h>

typedef unsigned short u16;
typedef __attribute__((ext_vector_type(8))) short bf16x8;
typedef __attribute__((ext_vector_type(4))) short s16x4;
typedef __attribute__((ext_vector_type(4))) float f32x4;
typedef __attribute__((ext_vector_type(16))) float f32x16;
typedef __attribute__((ext_vector_type(4))) unsigned u32x4;
typedef __attribute__((ext_vector_type(2))) unsigned u32x2;
typedef __attribute__((address_space(3))) s16x4* lds_s16x4_ptr;

#define DI __device__ __forceinline__

DI float bf2f(u16 v) { return __uint_as_float(((unsigned)v) << 16); }
DI u16 f2bf(float f) { __bf16 b = (__bf16)f; return __builtin_bit_cast(u16, b); }
DI float rbf(float f) { return bf2f(f2bf(f)); }

DI s16x4 tr_read(const u16* p) {
  return __builtin_amdgcn_ds_read_tr16_b64_v4i16((lds_s16x4_ptr)(p));
}

constexpr int S_ = 2048, D_ = 1024;

typedef __attribute__((address_space(3))) unsigned* lds_u32_ptr;
typedef const __attribute__((address_space(1))) unsigned* glb_u32_ptr;

DI void glds16(const void* gsrc, unsigned lds_dst) {
  unsigned keep;
  asm volatile("s_mov_b32 %0, m0\n\ts_mov_b32 m0, %2\n\ts_nop 0\n\tglobal_load_lds_dwordx4 %1, off\n\ts_mov_b32 m0, %0"
               : "=&s"(keep) : "v"(gsrc), "s"(lds_dst) : "memory");
}

template <int N>
DI void wait_vm() { asm volatile("s_waitcnt vmcnt(%0)" ::"i"(N) : "memory"); }

template <int BM, int BN, int WM, int WN, int MODE, int NST>
DI void gemm_body(char* lds, const int bid, const u16* __restrict__ A, int lda, const u16* __restrict__ B, int ldb,
                  u16* __restrict__ C, int ldc, int K) {
  constexpr int T = WM * WN * 64;
  constexpr int BK = 64;
  constexpr int ACH = BM * 8 / T;
  constexpr int BCPR = BN / 8;
  constexpr int BCH = BK * BCPR / T;
  constexpr int NL = ACH + BCH;
  constexpr int WTM = BM / WM, WTN = BN / WN;
  constexpr int MT = WTM / 16, NT = WTN / 16;
  constexpr int ABYTES = BM * 128, BBYTES = BK * BN * 2, STAGE = ABYTES + BBYTES;

  const int tid = threadIdx.x, lane = tid & 63, wid = tid >> 6;
  const int wm = wid / WN, wn = wid % WN;
  const int xcd = bid & 7, j = bid >> 3;
  const int bm = (xcd >> 1) * 4 + (j & 3);
  const int bn = (xcd & 1) * 8 + (j >> 2);
  const int brow = bm * BM, bcol = bn * BN;

  int aoff[ACH], boff[BCH];
#pragma unroll
  for (int i = 0; i < ACH; ++i) {
    int c = tid + i * T, r = c >> 3, cp = c & 7;
    aoff[i] = (brow + r) * lda + ((cp ^ ((r >> 1) & 7)) * 8);
  }
#pragma unroll
  for (int i = 0; i < BCH; ++i) {
    int c = tid + i * T, r = c / BCPR, cp = c % BCPR;
    int sw = ((((r >> 1) & 1) | (((r >> 3) & 1) << 1)) << 1);
    boff[i] = r * ldb + bcol + ((cp ^ sw) * 8);
  }
  const unsigned ldsbase = __builtin_amdgcn_readfirstlane((unsigned)(size_t)lds + wid * 1024);
  auto stage = [&](int kt, int buf) {
    const unsigned sA = ldsbase + buf * STAGE;
    const unsigned sB = sA + ABYTES;
#pragma unroll
    for (int i = 0; i < ACH; ++i) glds16(A + aoff[i] + kt * BK, sA + i * T * 16);
#pragma unroll
    for (int i = 0; i < BCH; ++i) glds16(B + boff[i] + (size_t)kt * BK * ldb, sB + i * T * 16);
  };

  f32x4 acc[MT][NT];
#pragma unroll
  for (int m = 0; m < MT; ++m)
#pragma unroll
    for (int n = 0; n < NT; ++n) acc[m][n] = (f32x4){0.f, 0.f, 0.f, 0.f};

  const int nt = K / BK;
#pragma unroll
  for (int i = 0; i < NST - 1; ++i) stage(i, i);
  const int g = lane >> 4, q = (lane & 15) >> 2, p = lane & 3;
  int aro[MT], bro[NT];
#pragma unroll
  for (int m = 0; m < MT; ++m) {
    int r = wm * WTM + m * 16 + (lane & 15);
    aro[m] = r * 128;
  }
  const int asw = (((wm * WTM + (lane & 15)) >> 1) & 7);
  const int brow0 = 8 * g + q;
  const int bsw = ((((brow0 >> 1) & 1) | (((brow0 >> 3) & 1) << 1)) << 1);
#pragma unroll
  for (int n = 0; n < NT; ++n) {
    int col = wn * WTN + n * 16 + 4 * p;
    bro[n] = brow0 * (BN * 2) + (((col >> 3) ^ bsw) * 16) + ((col >> 2) & 1) * 8;
  }
  int buf = 0;
  for (int kt = 0; kt < nt; ++kt) {
    if (NST == 3 && kt + 1 < nt) wait_vm<NL>(); else wait_vm<0>();
    __builtin_amdgcn_s_barrier();
    int nb = buf + NST - 1; if (nb >= NST) nb -= NST;
    if (kt + NST - 1 < nt) stage(kt + NST - 1, nb);
    const char* sA = lds + buf * STAGE;
    const char* sB = sA + ABYTES;
#pragma unroll
    for (int ks = 0; ks < 2; ++ks) {
      bf16x8 af[MT];
#pragma unroll
      for (int m = 0; m < MT; ++m)
        af[m] = *(const bf16x8*)(sA + aro[m] + (((ks * 4 + g) ^ asw) * 16));
      bf16x8 bfr[NT];
#pragma unroll
      for (int n = 0; n < NT; ++n) {
        const char* pb = sB + bro[n] + ks * 32 * (BN * 2);
        s16x4 lo = tr_read((const u16*)pb);
        s16x4 hi = tr_read((const u16*)(pb + 4 * (BN * 2)));
        bfr[n] = __builtin_shufflevector(lo, hi, 0, 1, 2, 3, 4, 5, 6, 7);
      }
#pragma unroll
      for (int m = 0; m < MT; ++m)
#pragma unroll
        for (int n = 0; n < NT; ++n)
          acc[m][n] = __builtin_amdgcn_mfma_f32_16x16x32_bf16(af[m], bfr[n], acc[m][n], 0, 0, 0);
    }
    buf = buf + 1; if (buf >= NST) buf = 0;
  }
  __syncthreads();
  constexpr int CS = BN + 8;
  u16* sC = (u16*)lds;
#pragma unroll
  for (int m = 0; m < MT; ++m)
#pragma unroll
    for (int n = 0; n < NT; ++n)
#pragma unroll
      for (int jj = 0; jj < 4; ++jj) {
        int r = wm * WTM + m * 16 + g * 4 + jj;
        int c = wn * WTN + n * 16 + (lane & 15);
        float t = rbf(acc[m][n][jj]);
        if (MODE == 1) {
          float r1 = (t > 0.f) ? rbf(t + 1.0f) : rbf(__expf(t));
          r1 = rbf(r1 + 1e-6f);
          t = (bcol + c < 2048) ? r1 : t;
        }
        sC[r * CS + c] = f2bf(t);
      }
  __syncthreads();
  for (int c = tid; c < BM * (BN / 8); c += T) {
    int r = c / (BN / 8), cc = c % (BN / 8);
    *(u32x4*)(C + (size_t)(brow + r) * ldc + bcol + cc * 8) = *(const u32x4*)(sC + r * CS + cc * 8);
  }
}

DI bf16x8 trfrag32(const u16* base, int stride, int ks, int lane) {
  const int h = lane >> 5, blk = (lane >> 4) & 1, q = (lane & 15) >> 2, p = lane & 3;
  const u16* pa = base + (16 * ks + 8 * h + q) * stride + 16 * blk + 4 * p;
  s16x4 lo = tr_read(pa);
  s16x4 hi = tr_read(pa + 4 * stride);
  return __builtin_shufflevector(lo, hi, 0, 1, 2, 3, 4, 5, 6, 7);
}

__global__ __launch_bounds__(256) void kvpart_k(const u16* __restrict__ qkv, float* __restrict__ part,
                                                float* __restrict__ zpart) {
  __shared__ __attribute__((aligned(16))) u16 spk[256 * 40];
  __shared__ __attribute__((aligned(16))) u16 sv[256 * 40];
  __shared__ float red[4 * 1056];
  const int h = blockIdx.x & 31, sp = blockIdx.x >> 5, t = threadIdx.x, lane = t & 63, w = t >> 6;
#pragma unroll
  for (int i = 0; i < 4; ++i) {
    int c = t + i * 256, r = c >> 2, cc = c & 3;
    const u16* rowp = qkv + (size_t)(sp * 256 + r) * 3072;
    *(u32x4*)(spk + r * 40 + cc * 8) = *(const u32x4*)(rowp + 1024 + h * 32 + cc * 8);
    *(u32x4*)(sv + r * 40 + cc * 8) = *(const u32x4*)(rowp + 2048 + h * 32 + cc * 8);
  }
  __syncthreads();
  f32x16 akv, az;
#pragma unroll
  for (int i = 0; i < 16; ++i) { akv[i] = 0.f; az[i] = 0.f; }
  bf16x8 ones;
#pragma unroll
  for (int i = 0; i < 8; ++i) ones[i] = (short)0x3f80;
#pragma unroll
  for (int ks = 0; ks < 4; ++ks) {
    bf16x8 a = trfrag32(spk + w * 64 * 40, 40, ks, lane);
    bf16x8 b = trfrag32(sv + w * 64 * 40, 40, ks, lane);
    akv = __builtin_amdgcn_mfma_f32_32x32x16_bf16(a, b, akv, 0, 0, 0);
    az = __builtin_amdgcn_mfma_f32_32x32x16_bf16(a, ones, az, 0, 0, 0);
  }
  const int n = lane & 31, hh = lane >> 5;
#pragma unroll
  for (int i = 0; i < 16; ++i) {
    int row = (i & 3) + 8 * (i >> 2) + 4 * hh;
    red[w * 1056 + row * 32 + n] = akv[i];
    if (n == 0) red[w * 1056 + 1024 + row] = az[i];
  }
  __syncthreads();
  for (int i = t; i < 1056; i += 256) {
    float s = red[i] + red[1056 + i] + red[2112 + i] + red[3168 + i];
    if (i < 1024) part[(size_t)(sp * 32 + h) * 1024 + i] = s;
    else zpart[(sp * 32 + h) * 32 + (i - 1024)] = s;
  }
}

__global__ __launch_bounds__(256) void o_k(const u16* __restrict__ qkv, const float* __restrict__ part,
                                           const float* __restrict__ zpart, u16* __restrict__ o) {
  __shared__ __attribute__((aligned(16))) u16 skvT[2 * 32 * 40];
  __shared__ __attribute__((aligned(16))) u16 sz[2 * 32];
  const int rb = blockIdx.x >> 4, hg = blockIdx.x & 15, t = threadIdx.x, lane = t & 63, w = t >> 6;
  for (int i = t; i < 2 * 1024; i += 256) {
    int hh = i >> 10, de = i & 1023;
    float s = 0;
#pragma unroll
    for (int sp = 0; sp < 8; ++sp) s += part[(size_t)(sp * 32 + hg * 2 + hh) * 1024 + de];
    skvT[hh * 1280 + (de & 31) * 40 + (de >> 5)] = f2bf(s);
  }
  if (t < 64) {
    int hh = t >> 5, d = t & 31;
    float s = 0;
#pragma unroll
    for (int sp = 0; sp < 8; ++sp) s += zpart[(sp * 32 + hg * 2 + hh) * 32 + d];
    sz[t] = f2bf(s);
  }
  __syncthreads();
  const int n = lane & 31, hf = lane >> 5;
  const int row0 = rb * 128 + w * 32;
#pragma unroll
  for (int hh = 0; hh < 2; ++hh) {
    f32x16 an, ad;
#pragma unroll
    for (int i = 0; i < 16; ++i) { an[i] = 0.f; ad[i] = 0.f; }
#pragma unroll
    for (int ks = 0; ks < 2; ++ks) {
      bf16x8 a = *(const bf16x8*)(qkv + (size_t)(row0 + n) * 3072 + (hg * 2 + hh) * 32 + ks * 16 + 8 * hf);
      bf16x8 b = *(const bf16x8*)(skvT + hh * 1280 + n * 40 + ks * 16 + 8 * hf);
      bf16x8 bz = *(const bf16x8*)(sz + hh * 32 + ks * 16 + 8 * hf);
      an = __builtin_amdgcn_mfma_f32_32x32x16_bf16(a, b, an, 0, 0, 0);
      ad = __builtin_amdgcn_mfma_f32_32x32x16_bf16(a, bz, ad, 0, 0, 0);
    }
#pragma unroll
    for (int i = 0; i < 16; ++i) {
      int row = (i & 3) + 8 * (i >> 2) + 4 * hf;
      float den = rbf(rbf(ad[i]) + 1e-6f);
      o[(size_t)(row0 + row) * 1024 + (hg * 2 + hh) * 32 + n] = f2bf(rbf(an[i]) / den);
    }
  }
}

__global__ __launch_bounds__(256) void pre_k(const u16* __restrict__ k, const u16* __restrict__ v,
                                             const u16* __restrict__ shk, u16* __restrict__ uT) {
  __shared__ __attribute__((aligned(16))) u16 tile[64 * 72];
  const int sb = blockIdx.x >> 4, db = blockIdx.x & 15, t = threadIdx.x;
  const int cc = t & 7, d0 = db * 64 + cc * 8;
  float sh[8][4];
#pragma unroll
  for (int i = 0; i < 8; ++i)
#pragma unroll
    for (int j = 0; j < 4; ++j) sh[i][j] = bf2f(shk[(d0 + i) * 4 + j]);
#pragma unroll
  for (int rr = 0; rr < 2; ++rr) {
    int r = (t >> 3) + rr * 32, s = sb * 64 + r;
    float acc[8];
#pragma unroll
    for (int i = 0; i < 8; ++i) acc[i] = 0.f;
#pragma unroll
    for (int tt = 0; tt < 4; ++tt) {
      int ss = s + tt - 3;
      if (ss >= 0) {
        u32x4 kk = *(const u32x4*)(k + (size_t)ss * D_ + d0);
#pragma unroll
        for (int i = 0; i < 8; ++i) {
          float kvv = (i & 1) ? __uint_as_float(kk[i >> 1] & 0xffff0000u) : __uint_as_float(kk[i >> 1] << 16);
          acc[i] = rbf(acc[i] + rbf(kvv * sh[i][3 - tt]));
        }
      }
    }
    u32x4 vv = *(const u32x4*)(v + (size_t)s * D_ + d0);
#pragma unroll
    for (int i = 0; i < 8; ++i) {
      float vf = (i & 1) ? __uint_as_float(vv[i >> 1] & 0xffff0000u) : __uint_as_float(vv[i >> 1] << 16);
      tile[(cc * 8 + i) * 72 + r] = f2bf(acc[i] * vf);
    }
  }
  __syncthreads();
  for (int c = t; c < 64 * 8; c += 256) {
    int d = c >> 3, c8 = c & 7;
    *(u32x4*)(uT + (size_t)(db * 64 + d) * S_ + sb * 64 + c8 * 8) = *(const u32x4*)(tile + d * 72 + c8 * 8);
  }
}

constexpr int WS = 328;
constexpr int HS = 2128;
constexpr int CW = 8 * WS + 64 * 72 + HS;
DI void wsync() { asm volatile("s_waitcnt lgkmcnt(0)" ::: "memory"); __builtin_amdgcn_wave_barrier(); }
DI void conv_body(u16* lds, const int bid, const u16* __restrict__ uT, const u16* __restrict__ hk,
                  u16* __restrict__ yT) {
  const int t = threadIdx.x, lane = t & 63, w = t >> 6;
  if (w >= 4) return;
  const int d = bid * 4 + w;
  u16* G = lds + w * CW;
  u16* U = G + 8 * WS;
  u16* HR = U + 64 * 72;
  const u16* h = hk + (size_t)d * S_;
#pragma unroll
  for (int i = 0; i < 4; ++i) {
    int c = lane + i * 64;
    u32x4 v = *(const u32x4*)(h + c * 8);
    u32x4 r;
#pragma unroll
    for (int j = 0; j < 4; ++j) r[j] = __builtin_amdgcn_alignbit(v[3 - j], v[3 - j], 16);
    *(u32x4*)(HR + 2040 - 8 * c) = r;
  }
  if (lane < 10) *(u32x4*)(HR + 2048 + lane * 8) = (u32x4){0u, 0u, 0u, 0u};
  for (int idx = lane; idx < 64 * 9; idx += 64) {
    int b = idx / 9, c8 = idx - b * 9;
    u32x4 val = (u32x4){0u, 0u, 0u, 0u};
    if (b >= 32 && c8 < 8) val = *(const u32x4*)(uT + (size_t)d * S_ + (b - 32) * 64 + c8 * 8);
    *(u32x4*)(U + b * 72 + c8 * 8) = val;
  }
  wsync();
  f32x16 acc0, acc1;
#pragma unroll
  for (int i = 0; i < 16; ++i) { acc0[i] = 0.f; acc1[i] = 0.f; }
  const int n = lane & 31, hh = lane >> 5;
  for (int m0 = 0; m0 < 32; m0 += 4) {
    const int xb = 1792 - 64 * m0;
    if (lane < 40) {
      u32x4 lo = *(const u32x4*)(HR + xb + 8 * lane);
      u32x4 hi = *(const u32x4*)(HR + xb + 8 * lane + 8);
      unsigned dd[8] = {lo[0], lo[1], lo[2], lo[3], hi[0], hi[1], hi[2], hi[3]};
#pragma unroll
      for (int e = 0; e < 4; ++e) {
        *(u32x4*)(G + (2 * e) * WS + 8 * lane) = (u32x4){dd[e], dd[e + 1], dd[e + 2], dd[e + 3]};
        u32x4 o;
#pragma unroll
        for (int k = 0; k < 4; ++k) o[k] = __builtin_amdgcn_alignbit(dd[e + k + 1], dd[e + k], 16);
        *(u32x4*)(G + (2 * e + 1) * WS + 8 * lane) = o;
      }
    }
    wsync();
#pragma unroll
    for (int mm = 0; mm < 4; ++mm) {
      const int m = m0 + mm;
#pragma unroll
      for (int kk = 0; kk < 4; ++kk) {
        bf16x8 bfr = *(const bf16x8*)(U + (n - m + 32) * 72 + kk * 16 + 8 * hh);
        int x0 = 2047 - 64 * m - n + kk * 16 + 8 * hh;
        int s0 = x0 & 7;
        const u16* gp = G + s0 * WS + (x0 - s0 - xb);
        bf16x8 a0 = *(const bf16x8*)(gp);
        bf16x8 a1 = *(const bf16x8*)(gp - 32);
        acc0 = __builtin_amdgcn_mfma_f32_32x32x16_bf16(a0, bfr, acc0, 0, 0, 0);
        acc1 = __builtin_amdgcn_mfma_f32_32x32x16_bf16(a1, bfr, acc1, 0, 0, 0);
      }
    }
    wsync();
  }
#pragma unroll
  for (int i = 0; i < 16; ++i) {
    int row = (i & 3) + 8 * (i >> 2) + 4 * hh;
    HR[n * 64 + row] = f2bf(acc0[i]);
    HR[n * 64 + 32 + row] = f2bf(acc1[i]);
  }
  wsync();
#pragma unroll
  for (int i = 0; i < 4; ++i) {
    int c = lane + i * 64;
    *(u32x4*)(yT + (size_t)d * S_ + c * 8) = *(const u32x4*)(HR + c * 8);
  }
}

DI void post_body(u16* lds, const int bid, const u16* __restrict__ uT, const u16* __restrict__ yT,
                  const u16* __restrict__ q, const u16* __restrict__ Dv, u16* __restrict__ out) {
  u16* tu = lds;
  u16* ty = lds + 64 * 72;
  const int sb = bid >> 4, db = bid & 15, t = threadIdx.x;
  for (int c = t; c < 64 * 8; c += 256) {
    int d = c >> 3, c8 = c & 7;
    *(u32x4*)(tu + d * 72 + c8 * 8) = *(const u32x4*)(uT + (size_t)(db * 64 + d) * S_ + sb * 64 + c8 * 8);
    *(u32x4*)(ty + d * 72 + c8 * 8) = *(const u32x4*)(yT + (size_t)(db * 64 + d) * S_ + sb * 64 + c8 * 8);
  }
  __syncthreads();
  const int cc = t & 7, d0 = db * 64 + cc * 8;
#pragma unroll
  for (int rr = 0; rr < 2; ++rr) {
    int r = (t >> 3) + rr * 32, s = sb * 64 + r;
    u32x4 qq = *(const u32x4*)(q + (size_t)s * D_ + d0);
    u16 ov[8];
#pragma unroll
    for (int i = 0; i < 8; ++i) {
      float qf = (i & 1) ? __uint_as_float(qq[i >> 1] & 0xffff0000u) : __uint_as_float(qq[i >> 1] << 16);
      float kvv = bf2f(tu[(cc * 8 + i) * 72 + r]);
      float y = bf2f(ty[(cc * 8 + i) * 72 + r]);
      float y2 = rbf(y + rbf(kvv * bf2f(Dv[d0 + i])));
      ov[i] = f2bf(qf * y2);
    }
    u32x4 w;
#pragma unroll
    for (int k = 0; k < 4; ++k) w[k] = (unsigned)ov[2 * k] | ((unsigned)ov[2 * k + 1] << 16);
    *(u32x4*)(out + (size_t)s * 2048 + 1024 + d0) = w;
  }
}

__global__ __launch_bounds__(512) void k_gemm1_conv(const u16* __restrict__ x, const u16* __restrict__ wqkv,
                                                    u16* __restrict__ qkv, const u16* __restrict__ uT,
                                                    const u16* __restrict__ ssk, u16* __restrict__ yT) {
  __shared__ __attribute__((aligned(1024))) char lds[2 * (128 * 128 + 64 * 192 * 2)];
  if (blockIdx.x < 256) gemm_body<128, 192, 2, 4, 1, 2>(lds, blockIdx.x, x, 1024, wqkv, 3072, qkv, 3072, 1024);
  else conv_body((u16*)lds, blockIdx.x - 256, uT, ssk, yT);
}
__global__ __launch_bounds__(256) void k_gemm2_post(const u16* __restrict__ o, const u16* __restrict__ wo,
                                                    u16* __restrict__ out, const u16* __restrict__ uT,
                                                    const u16* __restrict__ yT, const u16* __restrict__ q,
                                                    const u16* __restrict__ Dv) {
  __shared__ __attribute__((aligned(1024))) char lds[3 * (128 * 128 + 64 * 64 * 2)];
  if (blockIdx.x < 256) gemm_body<128, 64, 2, 2, 0, 3>(lds, blockIdx.x, o, 1024, wo, 1024, out, 2048, 1024);
  else post_body((u16*)lds, blockIdx.x - 256, uT, yT, q, Dv, out);
}

extern "C" void kernel_launch(void* const* d_in, const int* in_sizes, int n_in, void* d_out, int out_size,
                              void* d_ws, size_t ws_size, hipStream_t stream) {
  const u16* x = (const u16*)d_in[0];
  const u16* wqkv = (const u16*)d_in[1];
  const u16* wo = (const u16*)d_in[2];
  const u16* bq = (const u16*)d_in[3];
  const u16* bk = (const u16*)d_in[4];
  const u16* bv = (const u16*)d_in[5];
  const u16* shk = (const u16*)d_in[6];
  const u16* ssk = (const u16*)d_in[7];
  const u16* ssD = (const u16*)d_in[8];
  u16* out = (u16*)d_out;
  char* ws = (char*)d_ws;
  u16* qkv = (u16*)(ws);
  float* part = (float*)(ws + (12u << 20));
  float* zpart = (float*)(ws + (13u << 20));
  u16* o = (u16*)(ws + (14u << 20));
  u16* uT = (u16*)(ws + (18u << 20));
  u16* yT = (u16*)(ws + (22u << 20));

  pre_k<<<512, 256, 0, stream>>>(bk, bv, shk, uT);
  k_gemm1_conv<<<512, 512, 0, stream>>>(x, wqkv, qkv, uT, ssk, yT);
  kvpart_k<<<256, 256, 0, stream>>>(qkv, part, zpart);
  o_k<<<256, 256, 0, stream>>>(qkv, part, zpart, o);
  k_gemm2_post<<<768, 256, 0, stream>>>(o, wo, out, uT, yT, bq, ssD);
}
```

```cpp
#include <hip/hip_runtime.h>
#include <stdint.h>

typedef unsigned short u16;
typedef __attribute__((ext_vector_type(8))) short bf16x8;
typedef __attribute__((ext_vector_type(4))) short s16x4;
typedef __attribute__((ext_vector_type(4))) float f32x4;
typedef __attribute__((ext_vector_type(16))) float f32x16;
typedef __attribute__((ext_vector_type(4))) unsigned u32x4;
typedef __attribute__((ext_vector_type(2))) unsigned u32x2;
typedef __attribute__((address_space(3))) s16x4* lds_s16x4_ptr;

#define DI __device__ __forceinline__

DI float bf2f(u16 v) { return __uint_as_float(((unsigned)v) << 16); }
DI u16 f2bf(float f) { __bf16 b = (__bf16)f; return __builtin_bit_cast(u16, b); }
DI float rbf(float f) { return bf2f(f2bf(f)); }

DI s16x4 tr_read(const u16* p) {
  return __builtin_amdgcn_ds_read_tr16_b64_v4i16((lds_s16x4_ptr)(p));
}

constexpr int S_ = 2048, D_ = 1024;

typedef __attribute__((address_space(3))) unsigned* lds_u32_ptr;
typedef const __attribute__((address_space(1))) unsigned* glb_u32_ptr;

DI void glds16(const void* gsrc, unsigned lds_dst) {
  unsigned keep;
  asm volatile("s_mov_b32 %0, m0\n\ts_mov_b32 m0, %2\n\ts_nop 0\n\tglobal_load_lds_dwordx4 %1, off\n\ts_mov_b32 m0, %0"
               : "=&s"(keep) : "v"(gsrc), "s"(lds_dst) : "memory");
}

template <int N>
DI void wait_vm() { asm volatile("s_waitcnt vmcnt(%0)" ::"i"(N) : "memory"); }

template <int BM, int BN, int WM, int WN, int MODE, int NST>
DI void gemm_body(char* lds, const int bid, const u16* __restrict__ A, int lda, const u16* __restrict__ B, int ldb,
                  u16* __restrict__ C, int ldc, int K) {
  constexpr int T = WM * WN * 64;
  constexpr int BK = 64;
  constexpr int ACH = BM * 8 / T;
  constexpr int BCPR = BN / 8;
  constexpr int BCH = BK * BCPR / T;
  constexpr int NL = ACH + BCH;
  constexpr int WTM = BM / WM, WTN = BN / WN;
  constexpr int MT = WTM / 16, NT = WTN / 16;
  constexpr int ABYTES = BM * 128, BBYTES = BK * BN * 2, STAGE = ABYTES + BBYTES;

  const int tid = threadIdx.x, lane = tid & 63, wid = tid >> 6;
  const int wm = wid / WN, wn = wid % WN;
  const int xcd = bid & 7, j = bid >> 3;
  const int bm = (xcd >> 1) * 4 + (j & 3);
  const int bn = (xcd & 1) * 8 + (j >> 2);
  const int brow = bm * BM, bcol = bn * BN;

  int aoff[ACH], boff[BCH];
#pragma unroll
  for (int i = 0; i < ACH; ++i) {
    int c = tid + i * T, r = c >> 3, cp = c & 7;
    aoff[i] = (brow + r) * lda + ((cp ^ ((r >> 1) & 7)) * 8);
  }
#pragma unroll
  for (int i = 0; i < BCH; ++i) {
    int c = tid + i * T, r = c / BCPR, cp = c % BCPR;
    int sw = ((((r >> 1) & 1) | (((r >> 3) & 1) << 1)) << 1);
    boff[i] = r * ldb + bcol + ((cp ^ sw) * 8);
  }
  const unsigned ldsbase = __builtin_amdgcn_readfirstlane((unsigned)(size_t)lds + wid * 1024);
  auto stage = [&](int kt, int buf) {
    const unsigned sA = ldsbase + buf * STAGE;
    const unsigned sB = sA + ABYTES;
#pragma unroll
    for (int i = 0; i < ACH; ++i) glds16(A + aoff[i] + kt * BK, sA + i * T * 16);
#pragma unroll
    for (int i = 0; i < BCH; ++i) glds16(B + boff[i] + (size_t)kt * BK * ldb, sB + i * T * 16);
  };

  f32x4 acc[MT][NT];
#pragma unroll
  for (int m = 0; m < MT; ++m)
#pragma unroll
    for (int n = 0; n < NT; ++n) acc[m][n] = (f32x4){0.f, 0.f, 0.f, 0.f};

  const int nt = K / BK;
#pragma unroll
  for (int i = 0; i < NST - 1; ++i) stage(i, i);
  const int g = lane >> 4, q = (lane & 15) >> 2, p = lane & 3;
  int aro[MT], bro[NT];
#pragma unroll
  for (int m = 0; m < MT; ++m) {
    int r = wm * WTM + m * 16 + (lane & 15);
    aro[m] = r * 128;
  }
  const int asw = (((wm * WTM + (lane & 15)) >> 1) & 7);
  const int brow0 = 8 * g + q;
  const int bsw = ((((brow0 >> 1) & 1) | (((brow0 >> 3) & 1) << 1)) << 1);
#pragma unroll
  for (int n = 0; n < NT; ++n) {
    int col = wn * WTN + n * 16 + 4 * p;
    bro[n] = brow0 * (BN * 2) + (((col >> 3) ^ bsw) * 16) + ((col >> 2) & 1) * 8;
  }
  int buf = 0;
  for (int kt = 0; kt < nt; ++kt) {
    if (NST == 3 && kt + 1 < nt) wait_vm<NL>(); else wait_vm<0>();
    __builtin_amdgcn_s_barrier();
    int nb = buf + NST - 1; if (nb >= NST) nb -= NST;
    if (kt + NST - 1 < nt) stage(kt + NST - 1, nb);
    const char* sA = lds + buf * STAGE;
    const char* sB = sA + ABYTES;
#pragma unroll
    for (int ks = 0; ks < 2; ++ks) {
      bf16x8 af[MT];
#pragma unroll
      for (int m = 0; m < MT; ++m)
        af[m] = *(const bf16x8*)(sA + aro[m] + (((ks * 4 + g) ^ asw) * 16));
      bf16x8 bfr[NT];
#pragma unroll
      for (int n = 0; n < NT; ++n) {
        const char* pb = sB + bro[n] + ks * 32 * (BN * 2);
        s16x4 lo = tr_read((const u16*)pb);
        s16x4 hi = tr_read((const u16*)(pb + 4 * (BN * 2)));
        bfr[n] = __builtin_shufflevector(lo, hi, 0, 1, 2, 3, 4, 5, 6, 7);
      }
#pragma unroll
      for (int m = 0; m < MT; ++m)
#pragma unroll
        for (int n = 0; n < NT; ++n)
          acc[m][n] = __builtin_amdgcn_mfma_f32_16x16x32_bf16(af[m], bfr[n], acc[m][n], 0, 0, 0);
    }
    buf = buf + 1; if (buf >= NST) buf = 0;
  }
  __syncthreads();
  constexpr int CS = BN + 8;
  u16* sC = (u16*)lds;
#pragma unroll
  for (int m = 0; m < MT; ++m)
#pragma unroll
    for (int n = 0; n < NT; ++n)
#pragma unroll
      for (int jj = 0; jj < 4; ++jj) {
        int r = wm * WTM + m * 16 + g * 4 + jj;
        int c = wn * WTN + n * 16 + (lane & 15);
        float t = rbf(acc[m][n][jj]);
        if (MODE == 1) {
          float r1 = (t > 0.f) ? rbf(t + 1.0f) : rbf(__expf(t));
          r1 = rbf(r1 + 1e-6f);
          t = (bcol + c < 2048) ? r1 : t;
        }
        sC[r * CS + c] = f2bf(t);
      }
  __syncthreads();
  for (int c = tid; c < BM * (BN / 8); c += T) {
    int r = c / (BN / 8), cc = c % (BN / 8);
    *(u32x4*)(C + (size_t)(brow + r) * ldc + bcol + cc * 8) = *(const u32x4*)(sC + r * CS + cc * 8);
  }
}

DI bf16x8 trfrag32(const u16* base, int stride, int ks, int lane) {
  const int h = lane >> 5, blk = (lane >> 4) & 1, q = (lane & 15) >> 2, p = lane & 3;
  const u16* pa = base + (16 * ks + 8 * h + q) * stride + 16 * blk + 4 * p;
  s16x4 lo = tr_read(pa);
  s16x4 hi = tr_read(pa + 4 * stride);
  return __builtin_shufflevector(lo, hi, 0, 1, 2, 3, 4, 5, 6, 7);
}

DI void kvpart_body(u16* lds, const int bid, const u16* __restrict__ qkv, float* __restrict__ part,
                    float* __restrict__ zpart) {
  u16* spk = lds;
  u16* sv = lds + 256 * 40;
  float* red = (float*)(lds + 2 * 256 * 40);
  const int h = bid & 31, sp = bid >> 5, t = threadIdx.x, lane = t & 63, w = t >> 6;
#pragma unroll
  for (int i = 0; i < 4; ++i) {
    int c = t + i * 256, r = c >> 2, cc = c & 3;
    const u16* rowp = qkv + (size_t)(sp * 256 + r) * 3072;
    *(u32x4*)(spk + r * 40 + cc * 8) = *(const u32x4*)(rowp + 1024 + h * 32 + cc * 8);
    *(u32x4*)(sv + r * 40 + cc * 8) = *(const u32x4*)(rowp + 2048 + h * 32 + cc * 8);
  }
  __syncthreads();
  f32x16 akv, az;
#pragma unroll
  for (int i = 0; i < 16; ++i) { akv[i] = 0.f; az[i] = 0.f; }
  bf16x8 ones;
#pragma unroll
  for (int i = 0; i < 8; ++i) ones[i] = (short)0x3f80;
#pragma unroll
  for (int ks = 0; ks < 4; ++ks) {
    bf16x8 a = trfrag32(spk + w * 64 * 40, 40, ks, lane);
    bf16x8 b = trfrag32(sv + w * 64 * 40, 40, ks, lane);
    akv = __builtin_amdgcn_mfma_f32_32x32x16_bf16(a, b, akv, 0, 0, 0);
    az = __builtin_amdgcn_mfma_f32_32x32x16_bf16(a, ones, az, 0, 0, 0);
  }
  const int n = lane & 31, hh = lane >> 5;
#pragma unroll
  for (int i = 0; i < 16; ++i) {
    int row = (i & 3) + 8 * (i >> 2) + 4 * hh;
    red[w * 1056 + row * 32 + n] = akv[i];
    if (n == 0) red[w * 1056 + 1024 + row] = az[i];
  }
  __syncthreads();
  for (int i = t; i < 1056; i += 256) {
    float s = red[i] + red[1056 + i] + red[2112 + i] + red[3168 + i];
    if (i < 1024) part[(size_t)(sp * 32 + h) * 1024 + i] = s;
    else zpart[(sp * 32 + h) * 32 + (i - 1024)] = s;
  }
}

__global__ __launch_bounds__(256) void o_k(const u16* __restrict__ qkv, const float* __restrict__ part,
                                           const float* __restrict__ zpart, u16* __restrict__ o) {
  __shared__ __attribute__((aligned(16))) u16 skvT[2 * 32 * 40];
  __shared__ __attribute__((aligned(16))) u16 sz[2 * 32];
  const int rb = blockIdx.x >> 4, hg = blockIdx.x & 15, t = threadIdx.x, lane = t & 63, w = t >> 6;
  for (int i = t; i < 2 * 1024; i += 256) {
    int hh = i >> 10, de = i & 1023;
    float s = 0;
#pragma unroll
    for (int sp = 0; sp < 8; ++sp) s += part[(size_t)(sp * 32 + hg * 2 + hh) * 1024 + de];
    skvT[hh * 1280 + (de & 31) * 40 + (de >> 5)] = f2bf(s);
  }
  if (t < 64) {
    int hh = t >> 5, d = t & 31;
    float s = 0;
#pragma unroll
    for (int sp = 0; sp < 8; ++sp) s += zpart[(sp * 32 + hg * 2 + hh) * 32 + d];
    sz[t] = f2bf(s);
  }
  __syncthreads();
  const int n = lane & 31, hf = lane >> 5;
  const int row0 = rb * 128 + w * 32;
#pragma unroll
  for (int hh = 0; hh < 2; ++hh) {
    f32x16 an, ad;
#pragma unroll
    for (int i = 0; i < 16; ++i) { an[i] = 0.f; ad[i] = 0.f; }
#pragma unroll
    for (int ks = 0; ks < 2; ++ks) {
      bf16x8 a = *(const bf16x8*)(qkv + (size_t)(row0 + n) * 3072 + (hg * 2 + hh) * 32 + ks * 16 + 8 * hf);
      bf16x8 b = *(const bf16x8*)(skvT + hh * 1280 + n * 40 + ks * 16 + 8 * hf);
      bf16x8 bz = *(const bf16x8*)(sz + hh * 32 + ks * 16 + 8 * hf);
      an = __builtin_amdgcn_mfma_f32_32x32x16_bf16(a, b, an, 0, 0, 0);
      ad = __builtin_amdgcn_mfma_f32_32x32x16_bf16(a, bz, ad, 0, 0, 0);
    }
#pragma unroll
    for (int i = 0; i < 16; ++i) {
      int row = (i & 3) + 8 * (i >> 2) + 4 * hf;
      float den = rbf(rbf(ad[i]) + 1e-6f);
      o[(size_t)(row0 + row) * 1024 + (hg * 2 + hh) * 32 + n] = f2bf(rbf(an[i]) / den);
    }
  }
}

DI void pre_body(u16* tile, const int bid, const u16* __restrict__ k, const u16* __restrict__ v,
                 const u16* __restrict__ shk, u16* __restrict__ uT) {
  if (threadIdx.x >= 256) return;
  const int sb = bid >> 4, db = bid & 15, t = threadIdx.x;
  const int cc = t & 7, d0 = db * 64 + cc * 8;
  float sh[8][4];
#pragma unroll
  for (int i = 0; i < 8; ++i)
#pragma unroll
    for (int j = 0; j < 4; ++j) sh[i][j] = bf2f(shk[(d0 + i) * 4 + j]);
#pragma unroll
  for (int rr = 0; rr < 2; ++rr) {
    int r = (t >> 3) + rr * 32, s = sb * 64 + r;
    float acc[8];
#pragma unroll
    for (int i = 0; i < 8; ++i) acc[i] = 0.f;
#pragma unroll
    for (int tt = 0; tt < 4; ++tt) {
      int ss = s + tt - 3;
      if (ss >= 0) {
        u32x4 kk = *(const u32x4*)(k + (size_t)ss * D_ + d0);
#pragma unroll
        for (int i = 0; i < 8; ++i) {
          float kvv = (i & 1) ? __uint_as_float(kk[i >> 1] & 0xffff0000u) : __uint_as_float(kk[i >> 1] << 16);
          acc[i] = rbf(acc[i] + rbf(kvv * sh[i][3 - tt]));
        }
      }
    }
    u32x4 vv = *(const u32x4*)(v + (size_t)s * D_ + d0);
#pragma unroll
    for (int i = 0; i < 8; ++i) {
      float vf = (i & 1) ? __uint_as_float(vv[i >> 1] & 0xffff0000u) : __uint_as_float(vv[i >> 1] << 16);
      tile[(cc * 8 + i) * 72 + r] = f2bf(acc[i] * vf);
    }
  }
  __syncthreads();
  for (int c = t; c < 64 * 8; c += 256) {
    int d = c >> 3, c8 = c & 7;
    *(u32x4*)(uT + (size_t)(db * 64 + d) * S_ + sb * 64 + c8 * 8) = *(const u32x4*)(tile + d * 72 + c8 * 8);
  }
}

constexpr int WS = 328;
constexpr int HS = 2128;
constexpr int CW = 8 * WS + 64 * 72 + HS;
DI void wsync() { asm volatile("s_waitcnt lgkmcnt(0)" ::: "memory"); __builtin_amdgcn_wave_barrier(); }
DI void conv_body(u16* lds, const int bid, const u16* __restrict__ uT, const u16* __restrict__ hk,
                  u16* __restrict__ yT) {
  const int t = threadIdx.x, lane = t & 63, w = t >> 6;
  if (w >= 4) return;
  const int d = bid * 4 + w;
  u16* G = lds + w * CW;
  u16* U = G + 8 * WS;
  u16* HR = U + 64 * 72;
  const u16* h = hk + (size_t)d * S_;
#pragma unroll
  for (int i = 0; i < 4; ++i) {
    int c = lane + i * 64;
    u32x4 v = *(const u32x4*)(h + c * 8);
    u32x4 r;
#pragma unroll
    for (int j = 0; j < 4; ++j) r[j] = __builtin_amdgcn_alignbit(v[3 - j], v[3 - j], 16);
    *(u32x4*)(HR + 2040 - 8 * c) = r;
  }
  if (lane < 10) *(u32x4*)(HR + 2048 + lane * 8) = (u32x4){0u, 0u, 0u, 0u};
  for (int idx = lane; idx < 64 * 9; idx += 64) {
    int b = idx / 9, c8 = idx - b * 9;
    u32x4 val = (u32x4){0u, 0u, 0u, 0u};
    if (b >= 32 && c8 < 8) val = *(const u32x4*)(uT + (size_t)d * S_ + (b - 32) * 64 + c8 * 8);
    *(u32x4*)(U + b * 72 + c8 * 8) = val;
  }
  wsync();
  f32x16 acc0, acc1;
#pragma unroll
  for (int i = 0; i < 16; ++i) { acc0[i] = 0.f; acc1[i] = 0.f; }
  const int n = lane & 31, hh = lane >> 5;
  for (int m0 = 0; m0 < 32; m0 += 4) {
    const int xb = 1792 - 64 * m0;
    if (lane < 40) {
      u32x4 lo = *(const u32x4*)(HR + xb + 8 * lane);
      u32x4 hi = *(const u32x4*)(HR + xb + 8 * lane + 8);
      unsigned dd[8] = {lo[0], lo[1], lo[2], lo[3], hi[0], hi[1], hi[2], hi[3]};
#pragma unroll
      for (int e = 0; e < 4; ++e) {
        *(u32x4*)(G + (2 * e) * WS + 8 * lane) = (u32x4){dd[e], dd[e + 1], dd[e + 2], dd[e + 3]};
        u32x4 o;
#pragma unroll
        for (int k = 0; k < 4; ++k) o[k] = __builtin_amdgcn_alignbit(dd[e + k + 1], dd[e + k], 16);
        *(u32x4*)(G + (2 * e + 1) * WS + 8 * lane) = o;
      }
    }
    wsync();
#pragma unroll
    for (int mm = 0; mm < 4; ++mm) {
      const int m = m0 + mm;
#pragma unroll
      for (int kk = 0; kk < 4; ++kk) {
        bf16x8 bfr = *(const bf16x8*)(U + (n - m + 32) * 72 + kk * 16 + 8 * hh);
        int x0 = 2047 - 64 * m - n + kk * 16 + 8 * hh;
        int s0 = x0 & 7;
        const u16* gp = G + s0 * WS + (x0 - s0 - xb);
        bf16x8 a0 = *(const bf16x8*)(gp);
        bf16x8 a1 = *(const bf16x8*)(gp - 32);
        acc0 = __builtin_amdgcn_mfma_f32_32x32x16_bf16(a0, bfr, acc0, 0, 0, 0);
        acc1 = __builtin_amdgcn_mfma_f32_32x32x16_bf16(a1, bfr, acc1, 0, 0, 0);
      }
    }
    wsync();
  }
#pragma unroll
  for (int i = 0; i < 16; ++i) {
    int row = (i & 3) + 8 * (i >> 2) + 4 * hh;
    HR[n * 64 + row] = f2bf(acc0[i]);
    HR[n * 64 + 32 + row] = f2bf(acc1[i]);
  }
  wsync();
#pragma unroll
  for (int i = 0; i < 4; ++i) {
    int c = lane + i * 64;
    *(u32x4*)(yT + (size_t)d * S_ + c * 8) = *(const u32x4*)(HR + c * 8);
  }
}

DI void post_body(u16* lds, const int bid, const u16* __restrict__ uT, const u16* __restrict__ yT,
                  const u16* __restrict__ q, const u16* __restrict__ Dv, u16* __restrict__ out) {
  u16* tu = lds;
  u16* ty = lds + 64 * 72;
  const int sb = bid >> 4, db = bid & 15, t = threadIdx.x;
  for (int c = t; c < 64 * 8; c += 256) {
    int d = c >> 3, c8 = c & 7;
    *(u32x4*)(tu + d * 72 + c8 * 8) = *(const u32x4*)(uT + (size_t)(db * 64 + d) * S_ + sb * 64 + c8 * 8);
    *(u32x4*)(ty + d * 72 + c8 * 8) = *(const u32x4*)(yT + (size_t)(db * 64 + d) * S_ + sb * 64 + c8 * 8);
  }
  __syncthreads();
  const int cc = t & 7, d0 = db * 64 + cc * 8;
#pragma unroll
  for (int rr = 0; rr < 2; ++rr) {
    int r = (t >> 3) + rr * 32, s = sb * 64 + r;
    u32x4 qq = *(const u32x4*)(q + (size_t)s * D_ + d0);
    u16 ov[8];
#pragma unroll
    for (int i = 0; i < 8; ++i) {
      float qf = (i & 1) ? __uint_as_float(qq[i >> 1] & 0xffff0000u) : __uint_as_float(qq[i >> 1] << 16);
      float kvv = bf2f(tu[(cc * 8 + i) * 72 + r]);
      float y = bf2f(ty[(cc * 8 + i) * 72 + r]);
      float y2 = rbf(y + rbf(kvv * bf2f(Dv[d0 + i])));
      ov[i] = f2bf(qf * y2);
    }
    u32x4 w;
#pragma unroll
    for (int k = 0; k < 4; ++k) w[k] = (unsigned)ov[2 * k] | ((unsigned)ov[2 * k + 1] << 16);
    *(u32x4*)(out + (size_t)s * 2048 + 1024 + d0) = w;
  }
}

__global__ __launch_bounds__(512) void k_gemm1_pre(const u16* __restrict__ x, const u16* __restrict__ wqkv,
                                                   u16* __restrict__ qkv, const u16* __restrict__ bk,
                                                   const u16* __restrict__ bv, const u16* __restrict__ shk,
                                                   u16* __restrict__ uT) {
  __shared__ __attribute__((aligned(1024))) char lds[2 * (128 * 128 + 64 * 192 * 2)];
  if (blockIdx.x < 256) gemm_body<128, 192, 2, 4, 1, 2>(lds, blockIdx.x, x, 1024, wqkv, 3072, qkv, 3072, 1024);
  else pre_body((u16*)lds, blockIdx.x - 256, bk, bv, shk, uT);
}
__global__ __launch_bounds__(256) void k_kv_conv(const u16* __restrict__ qkv, float* __restrict__ part,
                                                 float* __restrict__ zpart, const u16* __restrict__ uT,
                                                 const u16* __restrict__ ssk, u16* __restrict__ yT) {
  __shared__ __attribute__((aligned(1024))) char lds[4 * CW * 2];
  if (blockIdx.x < 256) kvpart_body((u16*)lds, blockIdx.x, qkv, part, zpart);
  else conv_body((u16*)lds, blockIdx.x - 256, uT, ssk, yT);
}
__global__ __launch_bounds__(256) void k_gemm2_post(const u16* __restrict__ o, const u16* __restrict__ wo,
                                                    u16* __restrict__ out, const u16* __restrict__ uT,
                                                    const u16* __restrict__ yT, const u16* __restrict__ q,
                                                    const u16* __restrict__ Dv) {
  __shared__ __attribute__((aligned(1024))) char lds[3 * (128 * 128 + 64 * 64 * 2)];
  if (blockIdx.x < 256) gemm_body<128, 64, 2, 2, 0, 3>(lds, blockIdx.x, o, 1024, wo, 1024, out, 2048, 1024);
  else post_body((u16*)lds, blockIdx.x - 256, uT, yT, q, Dv, out);
}

extern "C" void kernel_launch(void* const* d_in, const int* in_sizes, int n_in, void* d_out, int out_size,
                              void* d_ws, size_t ws_size, hipStream_t stream) {
  const u16* x = (const u16*)d_in[0];
  const u16* wqkv = (const u16*)d_in[1];
  const u16* wo = (const u16*)d_in[2];
  const u16* bq = (const u16*)d_in[3];
  const u16* bk = (const u16*)d_in[4];
  const u16* bv = (const u16*)d_in[5];
  const u16* shk = (const u16*)d_in[6];
  const u16* ssk = (const u16*)d_in[7];
  const u16* ssD = (const u16*)d_in[8];
  u16* out = (u16*)d_out;
  char* ws = (char*)d_ws;
  u16* qkv = (u16*)(ws);
  float* part = (float*)(ws + (12u << 20));
  float* zpart = (float*)(ws + (13u << 20));
  u16* o = (u16*)(ws + (14u << 20));
  u16* uT = (u16*)(ws + (18u << 20));
  u16* yT = (u16*)(ws + (22u << 20));

  k_gemm1_pre<<<768, 512, 0, stream>>>(x, wqkv, qkv, bk, bv, shk, uT);
  k_kv_conv<<<512, 256, 0, stream>>>(qkv, part, zpart, uT, ssk, yT);
  o_k<<<256, 256, 0, stream>>>(qkv, part, zpart, o);
  k_gemm2_post<<<768, 256, 0, stream>>>(o, wo, out, uT, yT, bq, ssD);
}
```

```cpp
#include <hip/hip_runtime.h>
#include <stdint.h>

typedef unsigned short u16;
typedef __attribute__((ext_vector_type(8))) short bf16x8;
typedef __attribute__((ext_vector_type(4))) short s16x4;
typedef __attribute__((ext_vector_type(4))) float f32x4;
typedef __attribute__((ext_vector_type(16))) float f32x16;
typedef __attribute__((ext_vector_type(4))) unsigned u32x4;
typedef __attribute__((ext_vector_type(2))) unsigned u32x2;
typedef __attribute__((address_space(3))) s16x4* lds_s16x4_ptr;

#define DI __device__ __forceinline__

DI float bf2f(u16 v) { return __uint_as_float(((unsigned)v) << 16); }
DI u16 f2bf(float f) { __bf16 b = (__bf16)f; return __builtin_bit_cast(u16, b); }
DI float rbf(float f) { return bf2f(f2bf(f)); }

DI s16x4 tr_read(const u16* p) {
  return __builtin_amdgcn_ds_read_tr16_b64_v4i16((lds_s16x4_ptr)(p));
}

constexpr int S_ = 2048, D_ = 1024;

typedef __attribute__((address_space(3))) unsigned* lds_u32_ptr;
typedef const __attribute__((address_space(1))) unsigned* glb_u32_ptr;

DI void glds16(const void* gsrc, unsigned lds_dst) {
  unsigned keep;
  asm volatile("s_mov_b32 %0, m0\n\ts_mov_b32 m0, %2\n\ts_nop 0\n\tglobal_load_lds_dwordx4 %1, off\n\ts_mov_b32 m0, %0"
               : "=&s"(keep) : "v"(gsrc), "s"(lds_dst) : "memory");
}

template <int N>
DI void wait_vm() { asm volatile("s_waitcnt vmcnt(%0)" ::"i"(N) : "memory"); }

template <int BM, int BN, int WM, int WN, int MODE, int NST>
DI void gemm_body(char* lds, const int bid, const u16* __restrict__ A, int lda, const u16* __restrict__ B, int ldb,
                  u16* __restrict__ C, int ldc, int K) {
  constexpr int T = WM * WN * 64;
  constexpr int BK = 64;
  constexpr int ACH = BM * 8 / T;
  constexpr int BCPR = BN / 8;
  constexpr int BCH = BK * BCPR / T;
  constexpr int NL = ACH + BCH;
  constexpr int WTM = BM / WM, WTN = BN / WN;
  constexpr int MT = WTM / 16, NT = WTN / 16;
  constexpr int ABYTES = BM * 128, BBYTES = BK * BN * 2, STAGE = ABYTES + BBYTES;

  const int tid = threadIdx.x, lane = tid & 63, wid = tid >> 6;
  const int wm = wid / WN, wn = wid % WN;
  const int xcd = bid & 7, j = bid >> 3;
  const int bm = (xcd >> 1) * 4 + (j & 3);
  const int bn = (xcd & 1) * 8 + (j >> 2);
  const int brow = bm * BM, bcol = bn * BN;

  int aoff[ACH], boff[BCH];
#pragma unroll
  for (int i = 0; i < ACH; ++i) {
    int c = tid + i * T, r = c >> 3, cp = c & 7;
    aoff[i] = (brow + r) * lda + ((cp ^ ((r >> 1) & 7)) * 8);
  }
#pragma unroll
  for (int i = 0; i < BCH; ++i) {
    int c = tid + i * T, r = c / BCPR, cp = c % BCPR;
    int sw = ((((r >> 1) & 1) | (((r >> 3) & 1) << 1)) << 1);
    boff[i] = r * ldb + bcol + ((cp ^ sw) * 8);
  }
  const unsigned ldsbase = __builtin_amdgcn_readfirstlane((unsigned)(size_t)lds + wid * 1024);
  auto stage = [&](int kt, int buf) {
    const unsigned sA = ldsbase + buf * STAGE;
    const unsigned sB = sA + ABYTES;
#pragma unroll
    for (int i = 0; i < ACH; ++i) glds16(A + aoff[i] + kt * BK, sA + i * T * 16);
#pragma unroll
    for (int i = 0; i < BCH; ++i) glds16(B + boff[i] + (size_t)kt * BK * ldb, sB + i * T * 16);
  };

  f32x4 acc[MT][NT];
#pragma unroll
  for (int m = 0; m < MT; ++m)
#pragma unroll
    for (int n = 0; n < NT; ++n) acc[m][n] = (f32x4){0.f, 0.f, 0.f, 0.f};

  const int nt = K / BK;
#pragma unroll
  for (int i = 0; i < NST - 1; ++i) stage(i, i);
  const int g = lane >> 4, q = (lane & 15) >> 2, p = lane & 3;
  int aro[MT], bro[NT];
#pragma unroll
  for (int m = 0; m < MT; ++m) {
    int r = wm * WTM + m * 16 + (lane & 15);
    aro[m] = r * 128;
  }
  const int asw = (((wm * WTM + (lane & 15)) >> 1) & 7);
  const int brow0 = 8 * g + q;
  const int bsw = ((((brow0 >> 1) & 1) | (((brow0 >> 3) & 1) << 1)) << 1);
#pragma unroll
  for (int n = 0; n < NT; ++n) {
    int col = wn * WTN + n * 16 + 4 * p;
    bro[n] = brow0 * (BN * 2) + (((col >> 3) ^ bsw) * 16) + ((col >> 2) & 1) * 8;
  }
  int buf = 0;
  for (int kt = 0; kt < nt; ++kt) {
    if (NST == 3 && kt + 1 < nt) wait_vm<NL>(); else wait_vm<0>();
    __builtin_amdgcn_s_barrier();
    int nb = buf + NST - 1; if (nb >= NST) nb -= NST;
    if (kt + NST - 1 < nt) stage(kt + NST - 1, nb);
    const char* sA = lds + buf * STAGE;
    const char* sB = sA + ABYTES;
#pragma unroll
    for (int ks = 0; ks < 2; ++ks) {
      bf16x8 af[MT];
#pragma unroll
      for (int m = 0; m < MT; ++m)
        af[m] = *(const bf16x8*)(sA + aro[m] + (((ks * 4 + g) ^ asw) * 16));
      bf16x8 bfr[NT];
#pragma unroll
      for (int n = 0; n < NT; ++n) {
        const char* pb = sB + bro[n] + ks * 32 * (BN * 2);
        s16x4 lo = tr_read((const u16*)pb);
        s16x4 hi = tr_read((const u16*)(pb + 4 * (BN * 2)));
        bfr[n] = __builtin_shufflevector(lo, hi, 0, 1, 2, 3, 4, 5, 6, 7);
      }
#pragma unroll
      for (int m = 0; m < MT; ++m)
#pragma unroll
        for (int n = 0; n < NT; ++n)
          acc[m][n] = __builtin_amdgcn_mfma_f32_16x16x32_bf16(af[m], bfr[n], acc[m][n], 0, 0, 0);
    }
    buf = buf + 1; if (buf >= NST) buf = 0;
  }
  __syncthreads();
  constexpr int CS = BN + 8;
  u16* sC = (u16*)lds;
#pragma unroll
  for (int m = 0; m < MT; ++m)
#pragma unroll
    for (int n = 0; n < NT; ++n)
#pragma unroll
      for (int jj = 0; jj < 4; ++jj) {
        int r = wm * WTM + m * 16 + g * 4 + jj;
        int c = wn * WTN + n * 16 + (lane & 15);
        float t = rbf(acc[m][n][jj]);
        if (MODE == 1) {
          float r1 = (t > 0.f) ? rbf(t + 1.0f) : rbf(__expf(t));
          r1 = rbf(r1 + 1e-6f);
          t = (bcol + c < 2048) ? r1 : t;
        }
        sC[r * CS + c] = f2bf(t);
      }
  __syncthreads();
  for (int c = tid; c < BM * (BN / 8); c += T) {
    int r = c / (BN / 8), cc = c % (BN / 8);
    *(u32x4*)(C + (size_t)(brow + r) * ldc + bcol + cc * 8) = *(const u32x4*)(sC + r * CS + cc * 8);
  }
}

DI bf16x8 trfrag32(const u16* base, int stride, int ks, int lane) {
  const int h = lane >> 5, blk = (lane >> 4) & 1, q = (lane & 15) >> 2, p = lane & 3;
  const u16* pa = base + (16 * ks + 8 * h + q) * stride + 16 * blk + 4 * p;
  s16x4 lo = tr_read(pa);
  s16x4 hi = tr_read(pa + 4 * stride);
  return __builtin_shufflevector(lo, hi, 0, 1, 2, 3, 4, 5, 6, 7);
}

DI void kvpart_body(u16* lds, const int bid, const u16* __restrict__ qkv, float* __restrict__ part,
                    float* __restrict__ zpart) {
  u16* spk = lds;
  u16* sv = lds + 256 * 40;
  float* red = (float*)lds;
  const int h = bid & 31, sp = bid >> 5, t = threadIdx.x, lane = t & 63, w = t >> 6;
#pragma unroll
  for (int i = 0; i < 4; ++i) {
    int c = t + i * 256, r = c >> 2, cc = c & 3;
    const u16* rowp = qkv + (size_t)(sp * 256 + r) * 3072;
    *(u32x4*)(spk + r * 40 + cc * 8) = *(const u32x4*)(rowp + 1024 + h * 32 + cc * 8);
    *(u32x4*)(sv + r * 40 + cc * 8) = *(const u32x4*)(rowp + 2048 + h * 32 + cc * 8);
  }
  __syncthreads();
  f32x16 akv, az;
#pragma unroll
  for (int i = 0; i < 16; ++i) { akv[i] = 0.f; az[i] = 0.f; }
  bf16x8 ones;
#pragma unroll
  for (int i = 0; i < 8; ++i) ones[i] = (short)0x3f80;
#pragma unroll
  for (int ks = 0; ks < 4; ++ks) {
    bf16x8 a = trfrag32(spk + w * 64 * 40, 40, ks, lane);
    bf16x8 b = trfrag32(sv + w * 64 * 40, 40, ks, lane);
    akv = __builtin_amdgcn_mfma_f32_32x32x16_bf16(a, b, akv, 0, 0, 0);
    az = __builtin_amdgcn_mfma_f32_32x32x16_bf16(a, ones, az, 0, 0, 0);
  }
  const int n = lane & 31, hh = lane >> 5;
  __syncthreads();
#pragma unroll
  for (int i = 0; i < 16; ++i) {
    int row = (i & 3) + 8 * (i >> 2) + 4 * hh;
    red[w * 1056 + row * 32 + n] = akv[i];
    if (n == 0) red[w * 1056 + 1024 + row] = az[i];
  }
  __syncthreads();
  for (int i = t; i < 1056; i += 256) {
    float s = red[i] + red[1056 + i] + red[2112 + i] + red[3168 + i];
    if (i < 1024) part[(size_t)(sp * 32 + h) * 1024 + i] = s;
    else zpart[(sp * 32 + h) * 32 + (i - 1024)] = s;
  }
}

__global__ __launch_bounds__(256) void o_k(const u16* __restrict__ qkv, const float* __restrict__ part,
                                           const float* __restrict__ zpart, u16* __restrict__ o) {
  __shared__ __attribute__((aligned(16))) u16 skvT[2 * 32 * 40];
  __shared__ __attribute__((aligned(16))) u16 sz[2 * 32];
  const int rb = blockIdx.x >> 4, hg = blockIdx.x & 15, t = threadIdx.x, lane = t & 63, w = t >> 6;
  {
    f32x4 pv[2][8];
#pragma unroll
    for (int hh = 0; hh < 2; ++hh)
#pragma unroll
      for (int sp = 0; sp < 8; ++sp) pv[hh][sp] = *(const f32x4*)(part + (size_t)(sp * 32 + hg * 2 + hh) * 1024 + t * 4);
#pragma unroll
    for (int hh = 0; hh < 2; ++hh) {
      f32x4 sm = pv[hh][0];
#pragma unroll
      for (int sp = 1; sp < 8; ++sp) sm += pv[hh][sp];
      const int de = t * 4, dd = de >> 5, e0 = de & 31;
#pragma unroll
      for (int k = 0; k < 4; ++k) skvT[hh * 1280 + (e0 + k) * 40 + dd] = f2bf(sm[k]);
    }
  }
  if (t < 64) {
    int hh = t >> 5, d = t & 31;
    float s = 0;
#pragma unroll
    for (int sp = 0; sp < 8; ++sp) s += zpart[(sp * 32 + hg * 2 + hh) * 32 + d];
    sz[t] = f2bf(s);
  }
  __syncthreads();
  const int n = lane & 31, hf = lane >> 5;
  const int row0 = rb * 128 + w * 32;
#pragma unroll
  for (int hh = 0; hh < 2; ++hh) {
    f32x16 an, ad;
#pragma unroll
    for (int i = 0; i < 16; ++i) { an[i] = 0.f; ad[i] = 0.f; }
#pragma unroll
    for (int ks = 0; ks < 2; ++ks) {
      bf16x8 a = *(const bf16x8*)(qkv + (size_t)(row0 + n) * 3072 + (hg * 2 + hh) * 32 + ks * 16 + 8 * hf);
      bf16x8 b = *(const bf16x8*)(skvT + hh * 1280 + n * 40 + ks * 16 + 8 * hf);
      bf16x8 bz = *(const bf16x8*)(sz + hh * 32 + ks * 16 + 8 * hf);
      an = __builtin_amdgcn_mfma_f32_32x32x16_bf16(a, b, an, 0, 0, 0);
      ad = __builtin_amdgcn_mfma_f32_32x32x16_bf16(a, bz, ad, 0, 0, 0);
    }
#pragma unroll
    for (int i = 0; i < 16; ++i) {
      int row = (i & 3) + 8 * (i >> 2) + 4 * hf;
      float den = rbf(rbf(ad[i]) + 1e-6f);
      o[(size_t)(row0 + row) * 1024 + (hg * 2 + hh) * 32 + n] = f2bf(rbf(an[i]) / den);
    }
  }
}

DI void pre_body(u16* tile, const int bid, const u16* __restrict__ k, const u16* __restrict__ v,
                 const u16* __restrict__ shk, u16* __restrict__ uT) {
  if (threadIdx.x >= 256) return;
  const int sb = bid >> 4, db = bid & 15, t = threadIdx.x;
  const int cc = t & 7, d0 = db * 64 + cc * 8;
  float sh[8][4];
#pragma unroll
  for (int i = 0; i < 8; ++i)
#pragma unroll
    for (int j = 0; j < 4; ++j) sh[i][j] = bf2f(shk[(d0 + i) * 4 + j]);
#pragma unroll
  for (int rr = 0; rr < 2; ++rr) {
    int r = (t >> 3) + rr * 32, s = sb * 64 + r;
    float acc[8];
#pragma unroll
    for (int i = 0; i < 8; ++i) acc[i] = 0.f;
#pragma unroll
    for (int tt = 0; tt < 4; ++tt) {
      int ss = s + tt - 3;
      if (ss >= 0) {
        u32x4 kk = *(const u32x4*)(k + (size_t)ss * D_ + d0);
#pragma unroll
        for (int i = 0; i < 8; ++i) {
          float kvv = (i & 1) ? __uint_as_float(kk[i >> 1] & 0xffff0000u) : __uint_as_float(kk[i >> 1] << 16);
          acc[i] = rbf(acc[i] + rbf(kvv * sh[i][3 - tt]));
        }
      }
    }
    u32x4 vv = *(const u32x4*)(v + (size_t)s * D_ + d0);
#pragma unroll
    for (int i = 0; i < 8; ++i) {
      float vf = (i & 1) ? __uint_as_float(vv[i >> 1] & 0xffff0000u) : __uint_as_float(vv[i >> 1] << 16);
      tile[(cc * 8 + i) * 72 + r] = f2bf(acc[i] * vf);
    }
  }
  __syncthreads();
  for (int c = t; c < 64 * 8; c += 256) {
    int d = c >> 3, c8 = c & 7;
    *(u32x4*)(uT + (size_t)(db * 64 + d) * S_ + sb * 64 + c8 * 8) = *(const u32x4*)(tile + d * 72 + c8 * 8);
  }
}

constexpr int WS = 416;
constexpr int HS = 2128;
constexpr int CH = 33 * 72 + HS;
constexpr int CONV_LDS = (4 * 8 * WS + 2 * CH) * 2;
DI void wsync() { asm volatile("s_waitcnt lgkmcnt(0)" ::: "memory"); __builtin_amdgcn_wave_barrier(); }
DI void conv_body(u16* lds, const int bid, const u16* __restrict__ uT, const u16* __restrict__ hk,
                  u16* __restrict__ yT) {
  const int t = threadIdx.x, lane = t & 63, w = t >> 6;
  const int ch = w >> 1, par = w & 1;
  const int d = bid * 2 + ch;
  u16* G = lds + w * 8 * WS;
  u16* U = lds + 4 * 8 * WS + ch * CH;
  u16* HR = U + 33 * 72;
  {
    const u16* src = par ? (uT + (size_t)d * S_) : (hk + (size_t)d * S_);
    u32x4 gv[4];
#pragma unroll
    for (int i = 0; i < 4; ++i) gv[i] = *(const u32x4*)(src + (lane + i * 64) * 8);
    if (par) {
      if (lane < 9) *(u32x4*)(U + lane * 8) = (u32x4){0u, 0u, 0u, 0u};
#pragma unroll
      for (int i = 0; i < 4; ++i) {
        int c = lane + i * 64;
        *(u32x4*)(U + (1 + (c >> 3)) * 72 + (c & 7) * 8) = gv[i];
      }
    } else {
      if (lane < 10) *(u32x4*)(HR + 2048 + lane * 8) = (u32x4){0u, 0u, 0u, 0u};
#pragma unroll
      for (int i = 0; i < 4; ++i) {
        int c = lane + i * 64;
        u32x4 r;
#pragma unroll
        for (int j = 0; j < 4; ++j) r[j] = __builtin_amdgcn_alignbit(gv[i][3 - j], gv[i][3 - j], 16);
        *(u32x4*)(HR + 2040 - 8 * c) = r;
      }
    }
  }
  __syncthreads();
  f32x16 acc0, acc1;
#pragma unroll
  for (int i = 0; i < 16; ++i) { acc0[i] = 0.f; acc1[i] = 0.f; }
  const int n = lane & 31, hh = lane >> 5;
  for (int m0 = 4 * par; m0 < 32; m0 += 8) {
    const int xb = 1792 - 64 * m0;
    if (lane < 40) {
      u32x4 lo = *(const u32x4*)(HR + xb + 8 * lane);
      u32x4 hi = *(const u32x4*)(HR + xb + 8 * lane + 8);
      unsigned dd[8] = {lo[0], lo[1], lo[2], lo[3], hi[0], hi[1], hi[2], hi[3]};
#pragma unroll
      for (int e = 0; e < 4; ++e) {
        *(u32x4*)(G + (2 * e) * WS + 8 * lane) = (u32x4){dd[e], dd[e + 1], dd[e + 2], dd[e + 3]};
        u32x4 o;
#pragma unroll
        for (int k = 0; k < 4; ++k) o[k] = __builtin_amdgcn_alignbit(dd[e + k + 1], dd[e + k], 16);
        *(u32x4*)(G + (2 * e + 1) * WS + 8 * lane) = o;
      }
    }
    wsync();
    {
      const int s0 = (7 - n) & 7;
      const u16* gbase = G + s0 * WS + (2047 - n - s0 - 1792) + 8 * hh;
      const u16* ub[4];
#pragma unroll
      for (int mm = 0; mm < 4; ++mm) { int bi = n - m0 - mm; bi = bi < -1 ? -1 : bi; ub[mm] = U + (bi + 1) * 72 + 8 * hh; }
      bf16x8 fb[2][4], fa0[2][4], fa1[2][4];
#pragma unroll
      for (int kk = 0; kk < 4; ++kk) {
        fb[0][kk] = *(const bf16x8*)(ub[0] + 16 * kk);
        fa0[0][kk] = *(const bf16x8*)(gbase + 16 * kk);
        fa1[0][kk] = *(const bf16x8*)(gbase + 16 * kk - 32);
      }
#pragma unroll
      for (int mm = 0; mm < 4; ++mm) {
        const int cur = mm & 1, nx = cur ^ 1;
        if (mm < 3) {
#pragma unroll
          for (int kk = 0; kk < 4; ++kk) {
            fb[nx][kk] = *(const bf16x8*)(ub[mm + 1 < 4 ? mm + 1 : 3] + 16 * kk);
            fa0[nx][kk] = *(const bf16x8*)(gbase - 64 * (mm + 1) + 16 * kk);
            fa1[nx][kk] = *(const bf16x8*)(gbase - 64 * (mm + 1) + 16 * kk - 32);
          }
        }
        __builtin_amdgcn_sched_barrier(0);
#pragma unroll
        for (int kk = 0; kk < 4; ++kk) {
          acc0 = __builtin_amdgcn_mfma_f32_32x32x16_bf16(fa0[cur][kk], fb[cur][kk], acc0, 0, 0, 0);
          acc1 = __builtin_amdgcn_mfma_f32_32x32x16_bf16(fa1[cur][kk], fb[cur][kk], acc1, 0, 0, 0);
        }
      }
    }
    wsync();
  }
  __syncthreads();
  float* R = (float*)U;
  if (par) {
#pragma unroll
    for (int g4 = 0; g4 < 4; ++g4) {
      int row = 8 * g4 + 4 * hh;
      *(f32x4*)(R + n * 64 + row) = (f32x4){acc0[4 * g4], acc0[4 * g4 + 1], acc0[4 * g4 + 2], acc0[4 * g4 + 3]};
      *(f32x4*)(R + n * 64 + 32 + row) = (f32x4){acc1[4 * g4], acc1[4 * g4 + 1], acc1[4 * g4 + 2], acc1[4 * g4 + 3]};
    }
  }
  __syncthreads();
  if (!par) {
    u16* Y = G;
#pragma unroll
    for (int g4 = 0; g4 < 4; ++g4) {
      int row = 8 * g4 + 4 * hh;
      f32x4 p0 = *(const f32x4*)(R + n * 64 + row);
      f32x4 p1 = *(const f32x4*)(R + n * 64 + 32 + row);
      u32x2 w0, w1;
      w0[0] = (unsigned)f2bf(acc0[4 * g4] + p0[0]) | ((unsigned)f2bf(acc0[4 * g4 + 1] + p0[1]) << 16);
      w0[1] = (unsigned)f2bf(acc0[4 * g4 + 2] + p0[2]) | ((unsigned)f2bf(acc0[4 * g4 + 3] + p0[3]) << 16);
      w1[0] = (unsigned)f2bf(acc1[4 * g4] + p1[0]) | ((unsigned)f2bf(acc1[4 * g4 + 1] + p1[1]) << 16);
      w1[1] = (unsigned)f2bf(acc1[4 * g4 + 2] + p1[2]) | ((unsigned)f2bf(acc1[4 * g4 + 3] + p1[3]) << 16);
      *(u32x2*)(Y + n * 64 + row) = w0;
      *(u32x2*)(Y + n * 64 + 32 + row) = w1;
    }
    wsync();
#pragma unroll
    for (int i = 0; i < 4; ++i) {
      int c = lane + i * 64;
      *(u32x4*)(yT + (size_t)d * S_ + c * 8) = *(const u32x4*)(Y + c * 8);
    }
  }
}

DI void post_body(u16* lds, const int bid, const u16* __restrict__ uT, const u16* __restrict__ yT,
                  const u16* __restrict__ q, const u16* __restrict__ Dv, u16* __restrict__ out) {
  u16* tu = lds;
  u16* ty = lds + 64 * 72;
  const int sb = bid >> 4, db = bid & 15, t = threadIdx.x;
  for (int c = t; c < 64 * 8; c += 256) {
    int d = c >> 3, c8 = c & 7;
    *(u32x4*)(tu + d * 72 + c8 * 8) = *(const u32x4*)(uT + (size_t)(db * 64 + d) * S_ + sb * 64 + c8 * 8);
    *(u32x4*)(ty + d * 72 + c8 * 8) = *(const u32x4*)(yT + (size_t)(db * 64 + d) * S_ + sb * 64 + c8 * 8);
  }
  __syncthreads();
  const int cc = t & 7, d0 = db * 64 + cc * 8;
#pragma unroll
  for (int rr = 0; rr < 2; ++rr) {
    int r = (t >> 3) + rr * 32, s = sb * 64 + r;
    u32x4 qq = *(const u32x4*)(q + (size_t)s * D_ + d0);
    u16 ov[8];
#pragma unroll
    for (int i = 0; i < 8; ++i) {
      float qf = (i & 1) ? __uint_as_float(qq[i >> 1] & 0xffff0000u) : __uint_as_float(qq[i >> 1] << 16);
      float kvv = bf2f(tu[(cc * 8 + i) * 72 + r]);
      float y = bf2f(ty[(cc * 8 + i) * 72 + r]);
      float y2 = rbf(y + rbf(kvv * bf2f(Dv[d0 + i])));
      ov[i] = f2bf(qf * y2);
    }
    u32x4 w;
#pragma unroll
    for (int k = 0; k < 4; ++k) w[k] = (unsigned)ov[2 * k] | ((unsigned)ov[2 * k + 1] << 16);
    *(u32x4*)(out + (size_t)s * 2048 + 1024 + d0) = w;
  }
}

__global__ __launch_bounds__(512) void k_gemm1_pre(const u16* __restrict__ x, const u16* __restrict__ wqkv,
                                                   u16* __restrict__ qkv, const u16* __restrict__ bk,
                                                   const u16* __restrict__ bv, const u16* __restrict__ shk,
                                                   u16* __restrict__ uT) {
  __shared__ __attribute__((aligned(1024))) char lds[2 * (128 * 128 + 64 * 192 * 2)];
  if (blockIdx.x < 256) gemm_body<128, 192, 2, 4, 1, 2>(lds, blockIdx.x, x, 1024, wqkv, 3072, qkv, 3072, 1024);
  else pre_body((u16*)lds, blockIdx.x - 256, bk, bv, shk, uT);
}
__global__ __launch_bounds__(256) void k_kv_conv(const u16* __restrict__ qkv, float* __restrict__ part,
                                                 float* __restrict__ zpart, const u16* __restrict__ uT,
                                                 const u16* __restrict__ ssk, u16* __restrict__ yT) {
  __shared__ __attribute__((aligned(1024))) char lds[CONV_LDS];
  if (blockIdx.x < 256) kvpart_body((u16*)lds, blockIdx.x, qkv, part, zpart);
  else conv_body((u16*)lds, blockIdx.x - 256, uT, ssk, yT);
}
__global__ __launch_bounds__(256) void k_gemm2_post(const u16* __restrict__ o, const u16* __restrict__ wo,
                                                    u16* __restrict__ out, const u16* __restrict__ uT,
                                                    const u16* __restrict__ yT, const u16* __restrict__ q,
                                                    const u16* __restrict__ Dv) {
  __shared__ __attribute__((aligned(1024))) char lds[3 * (128 * 128 + 64 * 64 * 2)];
  if (blockIdx.x < 256) gemm_body<128, 64, 2, 2, 0, 3>(lds, blockIdx.x, o, 1024, wo, 1024, out, 2048, 1024);
  else post_body((u16*)lds, blockIdx.x - 256, uT, yT, q, Dv, out);
}

extern "C" void kernel_launch(void* const* d_in, const int* in_sizes, int n_in, void* d_out, int out_size,
                              void* d_ws, size_t ws_size, hipStream_t stream) {
  const u16* x = (const u16*)d_in[0];
  const u16* wqkv = (const u16*)d_in[1];
  const u16* wo = (const u16*)d_in[2];
  const u16* bq = (const u16*)d_in[3];
  const u16* bk = (const u16*)d_in[4];
  const u16* bv = (const u16*)d_in[5];
  const u16* shk = (const u16*)d_in[6];
  const u16* ssk = (const u16*)d_in[7];
  const u16* ssD = (const u16*)d_in[8];
  u16* out = (u16*)d_out;
  char* ws = (char*)d_ws;
  u16* qkv = (u16*)(ws);
  float* part = (float*)(ws + (12u << 20));
  float* zpart = (float*)(ws + (13u << 20));
  u16* o = (u16*)(ws + (14u << 20));
  u16* uT = (u16*)(ws + (18u << 20));
  u16* yT = (u16*)(ws + (22u << 20));

  k_gemm1_pre<<<768, 512, 0, stream>>>(x, wqkv, qkv, bk, bv, shk, uT);
  k_kv_conv<<<768, 256, 0, stream>>>(qkv, part, zpart, uT, ssk, yT);
  o_k<<<256, 256, 0, stream>>>(qkv, part, zpart, o);
  k_gemm2_post<<<768, 256, 0, stream>>>(o, wo, out, uT, yT, bq, ssD);
}
```

```cpp
#include <hip/hip_runtime.h>
#include <stdint.h>

typedef unsigned short u16;
typedef __attribute__((ext_vector_type(8))) short bf16x8;
typedef __attribute__((ext_vector_type(4))) short s16x4;
typedef __attribute__((ext_vector_type(4))) float f32x4;
typedef __attribute__((ext_vector_type(16))) float f32x16;
typedef __attribute__((ext_vector_type(4))) unsigned u32x4;
typedef __attribute__((ext_vector_type(2))) unsigned u32x2;
typedef __attribute__((address_space(3))) s16x4* lds_s16x4_ptr;

#define DI __device__ __forceinline__

DI float bf2f(u16 v) { return __uint_as_float(((unsigned)v) << 16); }
DI u16 f2bf(float f) { __bf16 b = (__bf16)f; return __builtin_bit_cast(u16, b); }
DI float rbf(float f) { return bf2f(f2bf(f)); }

DI s16x4 tr_read(const u16* p) {
  return __builtin_amdgcn_ds_read_tr16_b64_v4i16((lds_s16x4_ptr)(p));
}

constexpr int S_ = 2048, D_ = 1024;

typedef __attribute__((address_space(3))) unsigned* lds_u32_ptr;
typedef const __attribute__((address_space(1))) unsigned* glb_u32_ptr;

DI void glds16(const void* gsrc, unsigned lds_dst) {
  unsigned keep;
  asm volatile("s_mov_b32 %0, m0\n\ts_mov_b32 m0, %2\n\ts_nop 0\n\tglobal_load_lds_dwordx4 %1, off\n\ts_mov_b32 m0, %0"
               : "=&s"(keep) : "v"(gsrc), "s"(lds_dst) : "memory");
}

template <int N>
DI void wait_vm() { asm volatile("s_waitcnt vmcnt(%0)" ::"i"(N) : "memory"); }

template <int BM, int BN, int WM, int WN, int MODE, int NST>
DI void gemm_body(char* lds, const int bid, const u16* __restrict__ A, int lda, const u16* __restrict__ B, int ldb,
                  u16* __restrict__ C, int ldc, int K) {
  constexpr int T = WM * WN * 64;
  constexpr int BK = 64;
  constexpr int ACH = BM * 8 / T;
  constexpr int BCPR = BN / 8;
  constexpr int BCH = BK * BCPR / T;
  constexpr int NL = ACH + BCH;
  constexpr int WTM = BM / WM, WTN = BN / WN;
  constexpr int MT = WTM / 16, NT = WTN / 16;
  constexpr int ABYTES = BM * 128, BBYTES = BK * BN * 2, STAGE = ABYTES + BBYTES;

  const int tid = threadIdx.x, lane = tid & 63, wid = tid >> 6;
  const int wm = wid / WN, wn = wid % WN;
  const int xcd = bid & 7, j = bid >> 3;
  const int bm = (xcd >> 1) * 4 + (j & 3);
  const int bn = (xcd & 1) * 8 + (j >> 2);
  const int brow = bm * BM, bcol = bn * BN;

  int aoff[ACH], boff[BCH];
#pragma unroll
  for (int i = 0; i < ACH; ++i) {
    int c = tid + i * T, r = c >> 3, cp = c & 7;
    aoff[i] = (brow + r) * lda + ((cp ^ ((r >> 1) & 7)) * 8);
  }
#pragma unroll
  for (int i = 0; i < BCH; ++i) {
    int c = tid + i * T, r = c / BCPR, cp = c % BCPR;
    int sw = ((((r >> 1) & 1) | (((r >> 3) & 1) << 1)) << 1);
    boff[i] = r * ldb + bcol + ((cp ^ sw) * 8);
  }
  const unsigned ldsbase = __builtin_amdgcn_readfirstlane((unsigned)(size_t)lds + wid * 1024);
  auto stage = [&](int kt, int buf) {
    const unsigned sA = ldsbase + buf * STAGE;
    const unsigned sB = sA + ABYTES;
#pragma unroll
    for (int i = 0; i < ACH; ++i) glds16(A + aoff[i] + kt * BK, sA + i * T * 16);
#pragma unroll
    for (int i = 0; i < BCH; ++i) glds16(B + boff[i] + (size_t)kt * BK * ldb, sB + i * T * 16);
  };

  f32x4 acc[MT][NT];
#pragma unroll
  for (int m = 0; m < MT; ++m)
#pragma unroll
    for (int n = 0; n < NT; ++n) acc[m][n] = (f32x4){0.f, 0.f, 0.f, 0.f};

  const int nt = K / BK;
#pragma unroll
  for (int i = 0; i < NST - 1; ++i) stage(i, i);
  const int g = lane >> 4, q = (lane & 15) >> 2, p = lane & 3;
  int aro[MT], bro[NT];
#pragma unroll
  for (int m = 0; m < MT; ++m) {
    int r = wm * WTM + m * 16 + (lane & 15);
    aro[m] = r * 128;
  }
  const int asw = (((wm * WTM + (lane & 15)) >> 1) & 7);
  const int brow0 = 8 * g + q;
  const int bsw = ((((brow0 >> 1) & 1) | (((brow0 >> 3) & 1) << 1)) << 1);
#pragma unroll
  for (int n = 0; n < NT; ++n) {
    int col = wn * WTN + n * 16 + 4 * p;
    bro[n] = brow0 * (BN * 2) + (((col >> 3) ^ bsw) * 16) + ((col >> 2) & 1) * 8;
  }
  int buf = 0;
  for (int kt = 0; kt < nt; ++kt) {
    if (NST == 3 && kt + 1 < nt) wait_vm<NL>(); else wait_vm<0>();
    __builtin_amdgcn_s_barrier();
    int nb = buf + NST - 1; if (nb >= NST) nb -= NST;
    if (kt + NST - 1 < nt) stage(kt + NST - 1, nb);
    const char* sA = lds + buf * STAGE;
    const char* sB = sA + ABYTES;
#pragma unroll
    for (int ks = 0; ks < 2; ++ks) {
      bf16x8 af[MT];
#pragma unroll
      for (int m = 0; m < MT; ++m)
        af[m] = *(const bf16x8*)(sA + aro[m] + (((ks * 4 + g) ^ asw) * 16));
      bf16x8 bfr[NT];
#pragma unroll
      for (int n = 0; n < NT; ++n) {
        const char* pb = sB + bro[n] + ks * 32 * (BN * 2);
        s16x4 lo = tr_read((const u16*)pb);
        s16x4 hi = tr_read((const u16*)(pb + 4 * (BN * 2)));
        bfr[n] = __builtin_shufflevector(lo, hi, 0, 1, 2, 3, 4, 5, 6, 7);
      }
#pragma unroll
      for (int m = 0; m < MT; ++m)
#pragma unroll
        for (int n = 0; n < NT; ++n)
          acc[m][n] = __builtin_amdgcn_mfma_f32_16x16x32_bf16(af[m], bfr[n], acc[m][n], 0, 0, 0);
    }
    buf = buf + 1; if (buf >= NST) buf = 0;
  }
  __syncthreads();
  constexpr int CS = BN + 8;
  u16* sC = (u16*)lds;
#pragma unroll
  for (int m = 0; m < MT; ++m)
#pragma unroll
    for (int n = 0; n < NT; ++n)
#pragma unroll
      for (int jj = 0; jj < 4; ++jj) {
        int r = wm * WTM + m * 16 + g * 4 + jj;
        int c = wn * WTN + n * 16 + (lane & 15);
        float t = rbf(acc[m][n][jj]);
        if (MODE == 1) {
          float r1 = (t > 0.f) ? rbf(t + 1.0f) : rbf(__expf(t));
          r1 = rbf(r1 + 1e-6f);
          t = (bcol + c < 2048) ? r1 : t;
        }
        sC[r * CS + c] = f2bf(t);
      }
  __syncthreads();
  for (int c = tid; c < BM * (BN / 8); c += T) {
    int r = c / (BN / 8), cc = c % (BN / 8);
    *(u32x4*)(C + (size_t)(brow + r) * ldc + bcol + cc * 8) = *(const u32x4*)(sC + r * CS + cc * 8);
  }
}

DI bf16x8 trfrag32(const u16* base, int stride, int ks, int lane) {
  const int h = lane >> 5, blk = (lane >> 4) & 1, q = (lane & 15) >> 2, p = lane & 3;
  const u16* pa = base + (16 * ks + 8 * h + q) * stride + 16 * blk + 4 * p;
  s16x4 lo = tr_read(pa);
  s16x4 hi = tr_read(pa + 4 * stride);
  return __builtin_shufflevector(lo, hi, 0, 1, 2, 3, 4, 5, 6, 7);
}

DI void kvpart_body(u16* lds, const int bid, const u16* __restrict__ qkv, float* __restrict__ part,
                    float* __restrict__ zpart) {
  u16* spk = lds;
  u16* sv = lds + 256 * 40;
  float* red = (float*)(lds + 2 * 256 * 40);
  const int h = bid & 31, sp = bid >> 5, t = threadIdx.x, lane = t & 63, w = t >> 6;
#pragma unroll
  for (int i = 0; i < 4; ++i) {
    int c = t + i * 256, r = c >> 2, cc = c & 3;
    const u16* rowp = qkv + (size_t)(sp * 256 + r) * 3072;
    *(u32x4*)(spk + r * 40 + cc * 8) = *(const u32x4*)(rowp + 1024 + h * 32 + cc * 8);
    *(u32x4*)(sv + r * 40 + cc * 8) = *(const u32x4*)(rowp + 2048 + h * 32 + cc * 8);
  }
  __syncthreads();
  f32x16 akv, az;
#pragma unroll
  for (int i = 0; i < 16; ++i) { akv[i] = 0.f; az[i] = 0.f; }
  bf16x8 ones;
#pragma unroll
  for (int i = 0; i < 8; ++i) ones[i] = (short)0x3f80;
#pragma unroll
  for (int ks = 0; ks < 4; ++ks) {
    bf16x8 a = trfrag32(spk + w * 64 * 40, 40, ks, lane);
    bf16x8 b = trfrag32(sv + w * 64 * 40, 40, ks, lane);
    akv = __builtin_amdgcn_mfma_f32_32x32x16_bf16(a, b, akv, 0, 0, 0);
    az = __builtin_amdgcn_mfma_f32_32x32x16_bf16(a, ones, az, 0, 0, 0);
  }
  const int n = lane & 31, hh = lane >> 5;
#pragma unroll
  for (int i = 0; i < 16; ++i) {
    int row = (i & 3) + 8 * (i >> 2) + 4 * hh;
    red[w * 1056 + row * 32 + n] = akv[i];
    if (n == 0) red[w * 1056 + 1024 + row] = az[i];
  }
  __syncthreads();
  for (int i = t; i < 1056; i += 256) {
    float s = red[i] + red[1056 + i] + red[2112 + i] + red[3168 + i];
    if (i < 1024) part[(size_t)(sp * 32 + h) * 1024 + i] = s;
    else zpart[(sp * 32 + h) * 32 + (i - 1024)] = s;
  }
}

__global__ __launch_bounds__(256) void o_k(const u16* __restrict__ qkv, const float* __restrict__ part,
                                           const float* __restrict__ zpart, u16* __restrict__ o) {
  __shared__ __attribute__((aligned(16))) u16 skvT[2 * 32 * 40];
  __shared__ __attribute__((aligned(16))) u16 sz[2 * 32];
  const int rb = blockIdx.x >> 4, hg = blockIdx.x & 15, t = threadIdx.x, lane = t & 63, w = t >> 6;
  {
    f32x4 pv[2][8];
#pragma unroll
    for (int hh = 0; hh < 2; ++hh)
#pragma unroll
      for (int sp = 0; sp < 8; ++sp) pv[hh][sp] = *(const f32x4*)(part + (size_t)(sp * 32 + hg * 2 + hh) * 1024 + t * 4);
#pragma unroll
    for (int hh = 0; hh < 2; ++hh) {
      f32x4 sm = pv[hh][0];
#pragma unroll
      for (int sp = 1; sp < 8; ++sp) sm += pv[hh][sp];
      const int de = t * 4, dd = de >> 5, e0 = de & 31;
#pragma unroll
      for (int k = 0; k < 4; ++k) skvT[hh * 1280 + (e0 + k) * 40 + dd] = f2bf(sm[k]);
    }
  }
  if (t < 64) {
    int hh = t >> 5, d = t & 31;
    float s = 0;
#pragma unroll
    for (int sp = 0; sp < 8; ++sp) s += zpart[(sp * 32 + hg * 2 + hh) * 32 + d];
    sz[t] = f2bf(s);
  }
  __syncthreads();
  const int n = lane & 31, hf = lane >> 5;
  const int row0 = rb * 128 + w * 32;
#pragma unroll
  for (int hh = 0; hh < 2; ++hh) {
    f32x16 an, ad;
#pragma unroll
    for (int i = 0; i < 16; ++i) { an[i] = 0.f; ad[i] = 0.f; }
#pragma unroll
    for (int ks = 0; ks < 2; ++ks) {
      bf16x8 a = *(const bf16x8*)(qkv + (size_t)(row0 + n) * 3072 + (hg * 2 + hh) * 32 + ks * 16 + 8 * hf);
      bf16x8 b = *(const bf16x8*)(skvT + hh * 1280 + n * 40 + ks * 16 + 8 * hf);
      bf16x8 bz = *(const bf16x8*)(sz + hh * 32 + ks * 16 + 8 * hf);
      an = __builtin_amdgcn_mfma_f32_32x32x16_bf16(a, b, an, 0, 0, 0);
      ad = __builtin_amdgcn_mfma_f32_32x32x16_bf16(a, bz, ad, 0, 0, 0);
    }
#pragma unroll
    for (int i = 0; i < 16; ++i) {
      int row = (i & 3) + 8 * (i >> 2) + 4 * hf;
      float den = rbf(rbf(ad[i]) + 1e-6f);
      o[(size_t)(row0 + row) * 1024 + (hg * 2 + hh) * 32 + n] = f2bf(rbf(an[i]) / den);
    }
  }
}

DI void pre_body(u16* tile, const int bid, const u16* __restrict__ k, const u16* __restrict__ v,
                 const u16* __restrict__ shk, u16* __restrict__ uT) {
  if (threadIdx.x >= 256) return;
  const int sb = bid >> 4, db = bid & 15, t = threadIdx.x;
  const int cc = t & 7, d0 = db * 64 + cc * 8;
  float sh[8][4];
#pragma unroll
  for (int i = 0; i < 8; ++i)
#pragma unroll
    for (int j = 0; j < 4; ++j) sh[i][j] = bf2f(shk[(d0 + i) * 4 + j]);
#pragma unroll
  for (int rr = 0; rr < 2; ++rr) {
    int r = (t >> 3) + rr * 32, s = sb * 64 + r;
    float acc[8];
#pragma unroll
    for (int i = 0; i < 8; ++i) acc[i] = 0.f;
#pragma unroll
    for (int tt = 0; tt < 4; ++tt) {
      int ss = s + tt - 3;
      if (ss >= 0) {
        u32x4 kk = *(const u32x4*)(k + (size_t)ss * D_ + d0);
#pragma unroll
        for (int i = 0; i < 8; ++i) {
          float kvv = (i & 1) ? __uint_as_float(kk[i >> 1] & 0xffff0000u) : __uint_as_float(kk[i >> 1] << 16);
          acc[i] = rbf(acc[i] + rbf(kvv * sh[i][3 - tt]));
        }
      }
    }
    u32x4 vv = *(const u32x4*)(v + (size_t)s * D_ + d0);
#pragma unroll
    for (int i = 0; i < 8; ++i) {
      float vf = (i & 1) ? __uint_as_float(vv[i >> 1] & 0xffff0000u) : __uint_as_float(vv[i >> 1] << 16);
      tile[(cc * 8 + i) * 72 + r] = f2bf(acc[i] * vf);
    }
  }
  __syncthreads();
  for (int c = t; c < 64 * 8; c += 256) {
    int d = c >> 3, c8 = c & 7;
    *(u32x4*)(uT + (size_t)(db * 64 + d) * S_ + sb * 64 + c8 * 8) = *(const u32x4*)(tile + d * 72 + c8 * 8);
  }
}

constexpr int WS = 416;
constexpr int HS = 2128;
constexpr int CW = 8 * WS + 33 * 72 + HS;
DI void wsync() { asm volatile("s_waitcnt lgkmcnt(0)" ::: "memory"); __builtin_amdgcn_wave_barrier(); }
DI void conv_body(u16* lds, const int bid, const u16* __restrict__ uT, const u16* __restrict__ hk,
                  u16* __restrict__ yT) {
  const int t = threadIdx.x, lane = t & 63, w = t >> 6;
  if (w >= 4) return;
  const int d = bid * 4 + w;
  u16* G = lds + w * CW;
  u16* U = G + 8 * WS;
  u16* HR = U + 33 * 72;
  const u16* h = hk + (size_t)d * S_;
  u32x4 hv[4], uv[4];
#pragma unroll
  for (int i = 0; i < 4; ++i) {
    hv[i] = *(const u32x4*)(h + (lane + i * 64) * 8);
    uv[i] = *(const u32x4*)(uT + (size_t)d * S_ + (lane + i * 64) * 8);
  }
  if (lane < 9) *(u32x4*)(U + lane * 8) = (u32x4){0u, 0u, 0u, 0u};
  if (lane < 10) *(u32x4*)(HR + 2048 + lane * 8) = (u32x4){0u, 0u, 0u, 0u};
#pragma unroll
  for (int i = 0; i < 4; ++i) {
    int c = lane + i * 64;
    u32x4 r;
#pragma unroll
    for (int j = 0; j < 4; ++j) r[j] = __builtin_amdgcn_alignbit(hv[i][3 - j], hv[i][3 - j], 16);
    *(u32x4*)(HR + 2040 - 8 * c) = r;
    *(u32x4*)(U + (1 + (c >> 3)) * 72 + (c & 7) * 8) = uv[i];
  }
  wsync();
  f32x16 acc0, acc1;
#pragma unroll
  for (int i = 0; i < 16; ++i) { acc0[i] = 0.f; acc1[i] = 0.f; }
  const int n = lane & 31, hh = lane >> 5;
  for (int m0 = 0; m0 < 32; m0 += 4) {
    const int xb = 1792 - 64 * m0;
    if (lane < 40) {
      u32x4 lo = *(const u32x4*)(HR + xb + 8 * lane);
      u32x4 hi = *(const u32x4*)(HR + xb + 8 * lane + 8);
      unsigned dd[8] = {lo[0], lo[1], lo[2], lo[3], hi[0], hi[1], hi[2], hi[3]};
#pragma unroll
      for (int e = 0; e < 4; ++e) {
        *(u32x4*)(G + (2 * e) * WS + 8 * lane) = (u32x4){dd[e], dd[e + 1], dd[e + 2], dd[e + 3]};
        u32x4 o;
#pragma unroll
        for (int k = 0; k < 4; ++k) o[k] = __builtin_amdgcn_alignbit(dd[e + k + 1], dd[e + k], 16);
        *(u32x4*)(G + (2 * e + 1) * WS + 8 * lane) = o;
      }
    }
    wsync();
    {
      const int s0 = (7 - n) & 7;
      const u16* gbase = G + s0 * WS + (2047 - n - s0 - 1792) + 8 * hh;
      const u16* ub[4];
#pragma unroll
      for (int mm = 0; mm < 4; ++mm) { int bi = n - m0 - mm; bi = bi < -1 ? -1 : bi; ub[mm] = U + (bi + 1) * 72 + 8 * hh; }
      bf16x8 fb[2][4], fa0[2][4], fa1[2][4];
#pragma unroll
      for (int kk = 0; kk < 4; ++kk) {
        fb[0][kk] = *(const bf16x8*)(ub[0] + 16 * kk);
        fa0[0][kk] = *(const bf16x8*)(gbase + 16 * kk);
        fa1[0][kk] = *(const bf16x8*)(gbase + 16 * kk - 32);
      }
#pragma unroll
      for (int mm = 0; mm < 4; ++mm) {
        const int cur = mm & 1, nx = cur ^ 1;
        if (mm < 3) {
#pragma unroll
          for (int kk = 0; kk < 4; ++kk) {
            fb[nx][kk] = *(const bf16x8*)(ub[mm + 1 < 4 ? mm + 1 : 3] + 16 * kk);
            fa0[nx][kk] = *(const bf16x8*)(gbase - 64 * (mm + 1) + 16 * kk);
            fa1[nx][kk] = *(const bf16x8*)(gbase - 64 * (mm + 1) + 16 * kk - 32);
          }
        }
        __builtin_amdgcn_sched_barrier(0);
#pragma unroll
        for (int kk = 0; kk < 4; ++kk) {
          acc0 = __builtin_amdgcn_mfma_f32_32x32x16_bf16(fa0[cur][kk], fb[cur][kk], acc0, 0, 0, 0);
          acc1 = __builtin_amdgcn_mfma_f32_32x32x16_bf16(fa1[cur][kk], fb[cur][kk], acc1, 0, 0, 0);
        }
      }
    }
    wsync();
  }
#pragma unroll
  for (int i = 0; i < 16; ++i) {
    int row = (i & 3) + 8 * (i >> 2) + 4 * hh;
    HR[n * 64 + row] = f2bf(acc0[i]);
    HR[n * 64 + 32 + row] = f2bf(acc1[i]);
  }
  wsync();
#pragma unroll
  for (int i = 0; i < 4; ++i) {
    int c = lane + i * 64;
    *(u32x4*)(yT + (size_t)d * S_ + c * 8) = *(const u32x4*)(HR + c * 8);
  }
}

DI void post_body(u16* lds, const int bid, const u16* __restrict__ uT, const u16* __restrict__ yT,
                  const u16* __restrict__ q, const u16* __restrict__ Dv, u16* __restrict__ out) {
  u16* tu = lds;
  u16* ty = lds + 64 * 72;
  const int sb = bid >> 4, db = bid & 15, t = threadIdx.x;
  for (int c = t; c < 64 * 8; c += 256) {
    int d = c >> 3, c8 = c & 7;
    *(u32x4*)(tu + d * 72 + c8 * 8) = *(const u32x4*)(uT + (size_t)(db * 64 + d) * S_ + sb * 64 + c8 * 8);
    *(u32x4*)(ty + d * 72 + c8 * 8) = *(const u32x4*)(yT + (size_t)(db * 64 + d) * S_ + sb * 64 + c8 * 8);
  }
  __syncthreads();
  const int cc = t & 7, d0 = db * 64 + cc * 8;
#pragma unroll
  for (int rr = 0; rr < 2; ++rr) {
    int r = (t >> 3) + rr * 32, s = sb * 64 + r;
    u32x4 qq = *(const u32x4*)(q + (size_t)s * D_ + d0);
    u16 ov[8];
#pragma unroll
    for (int i = 0; i < 8; ++i) {
      float qf = (i & 1) ? __uint_as_float(qq[i >> 1] & 0xffff0000u) : __uint_as_float(qq[i >> 1] << 16);
      float kvv = bf2f(tu[(cc * 8 + i) * 72 + r]);
      float y = bf2f(ty[(cc * 8 + i) * 72 + r]);
      float y2 = rbf(y + rbf(kvv * bf2f(Dv[d0 + i])));
      ov[i] = f2bf(qf * y2);
    }
    u32x4 w;
#pragma unroll
    for (int k = 0; k < 4; ++k) w[k] = (unsigned)ov[2 * k] | ((unsigned)ov[2 * k + 1] << 16);
    *(u32x4*)(out + (size_t)s * 2048 + 1024 + d0) = w;
  }
}

__global__ __launch_bounds__(512) void k_gemm1_pre(const u16* __restrict__ x, const u16* __restrict__ wqkv,
                                                   u16* __restrict__ qkv, const u16* __restrict__ bk,
                                                   const u16* __restrict__ bv, const u16* __restrict__ shk,
                                                   u16* __restrict__ uT) {
  __shared__ __attribute__((aligned(1024))) char lds[2 * (128 * 128 + 64 * 192 * 2)];
  if (blockIdx.x < 256) gemm_body<128, 192, 2, 4, 1, 2>(lds, blockIdx.x, x, 1024, wqkv, 3072, qkv, 3072, 1024);
  else pre_body((u16*)lds, blockIdx.x - 256, bk, bv, shk, uT);
}
__global__ __launch_bounds__(256) void k_kv_conv(const u16* __restrict__ qkv, float* __restrict__ part,
                                                 float* __restrict__ zpart, const u16* __restrict__ uT,
                                                 const u16* __restrict__ ssk, u16* __restrict__ yT) {
  __shared__ __attribute__((aligned(1024))) char lds[4 * CW * 2];
  if (blockIdx.x < 256) kvpart_body((u16*)lds, blockIdx.x, qkv, part, zpart);
  else conv_body((u16*)lds, blockIdx.x - 256, uT, ssk, yT);
}
__global__ __launch_bounds__(256) void k_gemm2_post(const u16* __restrict__ o, const u16* __restrict__ wo,
                                                    u16* __restrict__ out, const u16* __restrict__ uT,
                                                    const u16* __restrict__ yT, const u16* __restrict__ q,
                                                    const u16* __restrict__ Dv) {
  __shared__ __attribute__((aligned(1024))) char lds[3 * (128 * 128 + 64 * 64 * 2)];
  if (blockIdx.x < 256) gemm_body<128, 64, 2, 2, 0, 3>(lds, blockIdx.x, o, 1024, wo, 1024, out, 2048, 1024);
  else post_body((u16*)lds, blockIdx.x - 256, uT, yT, q, Dv, out);
}

extern "C" void kernel_launch(void* const* d_in, const int* in_sizes, int n_in, void* d_out, int out_size,
                              void* d_ws, size_t ws_size, hipStream_t stream) {
  const u16* x = (const u16*)d_in[0];
  const u16* wqkv = (const u16*)d_in[1];
  const u16* wo = (const u16*)d_in[2];
  const u16* bq = (const u16*)d_in[3];
  const u16* bk = (const u16*)d_in[4];
  const u16* bv = (const u16*)d_in[5];
  const u16* shk = (const u16*)d_in[6];
  const u16* ssk = (const u16*)d_in[7];
  const u16* ssD = (const u16*)d_in[8];
  u16* out = (u16*)d_out;
  char* ws = (char*)d_ws;
  u16* qkv = (u16*)(ws);
  float* part = (float*)(ws + (12u << 20));
  float* zpart = (float*)(ws + (13u << 20));
  u16* o = (u16*)(ws + (14u << 20));
  u16* uT = (u16*)(ws + (18u << 20));
  u16* yT = (u16*)(ws + (22u << 20));

  k_gemm1_pre<<<768, 512, 0, stream>>>(x, wqkv, qkv, bk, bv, shk, uT);
  k_kv_conv<<<512, 256, 0, stream>>>(qkv, part, zpart, uT, ssk, yT);
  o_k<<<256, 256, 0, stream>>>(qkv, part, zpart, o);
  k_gemm2_post<<<768, 256, 0, stream>>>(o, wo, out, uT, yT, bq, ssD);
}
```
